```python
import torch

def _softmax(x, axis=-1):
    return torch.softmax(x, dim=axis)
def _gelu(x):
    return torch.nn.functional.gelu(x, approximate="tanh")
def _silu(x):
    return torch.nn.functional.silu(x)
def _sigmoid(x):
    return torch.sigmoid(x)
def _layernorm(x, axis=-1, eps=1e-5):
    m = x.mean(dim=axis, keepdim=True)
    v = x.var(dim=axis, keepdim=True, unbiased=False)
    return (x - m) / torch.sqrt(v + eps)
def _scatter_add(tgt, idx, src, axis=0):
    return tgt.clone().index_add_(axis, idx, src)
def _segment_sum(src, seg, n):
    out = torch.zeros((n,) + src.shape[1:], dtype=src.dtype)
    return out.index_add_(0, seg, src)
def _pad(x, pw):
    flat = tuple(v for pair in reversed(pw) for v in pair)
    return torch.nn.functional.pad(x, flat)

kernel_name = 'standalone__batchnorm__sampling'

def setup_inputs():

    _w = (lambda a: (lambda t: t.to(torch.bfloat16) if t.is_floating_point() else t)(torch.as_tensor(a)))
    B, S, C = 1024, 16, 448
    K = 3
    return {
        "x": _w((lambda *s: torch.randn(*s, dtype=torch.bfloat16) * 0.05)(B, S, C)),
        "w": _w((lambda *s: torch.randn(*s, dtype=torch.bfloat16) * 0.05)(K, C, C) * (1.0 / (K * C) ** 0.5)),
        "mean": _w((lambda *s: torch.randn(*s, dtype=torch.bfloat16) * 0.05)(C) * 0.1),
        "var": _w(torch.abs((lambda *s: torch.randn(*s, dtype=torch.bfloat16) * 0.05)(C)) + 0.5),
        "g": _w((lambda *s: torch.randn(*s, dtype=torch.bfloat16) * 0.05)(C) + 1.0),
        "b": _w((lambda *s: torch.randn(*s, dtype=torch.bfloat16) * 0.05)(C) * 0.1),
    }

def reference(x, w, mean, var, g, b):
    K = 3
    P = K // 2
    S = x.shape[1]
    xp = _pad(x, ((0, 0), (P, P), (0, 0)))
    y = sum(torch.einsum('bsc,cd->bsd', xp[:, k:k + S, :], w[k]) for k in range(K))
    inv_std = 1.0 / torch.sqrt(var + 1e-5)
    y = (y - mean) * (g * inv_std) + b
    return _silu(y)
```

```cpp
#include <hip/hip_runtime.h>
#include <hip/hip_bf16.h>
#include <stdint.h>

static __device__ __forceinline__ float bf2f(unsigned short u) { return __uint_as_float(((unsigned)u) << 16); }

__global__ void naive_kernel(const unsigned short* __restrict__ x, const unsigned short* __restrict__ w,
                             const unsigned short* __restrict__ mean, const unsigned short* __restrict__ var,
                             const unsigned short* __restrict__ g, const unsigned short* __restrict__ b,
                             __hip_bfloat16* __restrict__ out) {
    const int C = 448, S = 16;
    int row = blockIdx.x;
    int s = row & 15;
    int d = threadIdx.x;
    __shared__ float xs[3][448];
    for (int k = 0; k < 3; ++k) {
        int ss = s + k - 1;
        xs[k][d] = (ss >= 0 && ss < S) ? bf2f(x[(size_t)(row + k - 1) * C + d]) : 0.f;
    }
    __syncthreads();
    float acc = 0.f;
    for (int k = 0; k < 3; ++k)
        for (int c = 0; c < C; ++c)
            acc += xs[k][c] * bf2f(w[((size_t)k * C + c) * C + d]);
    float inv = 1.0f / sqrtf(bf2f(var[d]) + 1e-5f);
    float y = (acc - bf2f(mean[d])) * (bf2f(g[d]) * inv) + bf2f(b[d]);
    float o = y / (1.0f + __expf(-y));
    out[(size_t)row * C + d] = __float2bfloat16(o);
}

extern "C" void kernel_launch(void* const* d_in, const int* in_sizes, int n_in,
                              void* d_out, int out_size, void* d_ws, size_t ws_size,
                              hipStream_t stream) {
    naive_kernel<<<16384, 448, 0, stream>>>((const unsigned short*)d_in[0], (const unsigned short*)d_in[1],
        (const unsigned short*)d_in[2], (const unsigned short*)d_in[3], (const unsigned short*)d_in[4],
        (const unsigned short*)d_in[5], (__hip_bfloat16*)d_out);
}
```

```cpp
#include <hip/hip_runtime.h>
#include <hip/hip_bf16.h>
#include <stdint.h>

typedef unsigned u32x4 __attribute__((ext_vector_type(4)));

__global__ __launch_bounds__(256, 1) void conv_bn_silu_kernel(const char* __restrict__ x, const char* __restrict__ w,
                                                              const char* __restrict__ mean, const char* __restrict__ var,
                                                              const char* __restrict__ g, const char* __restrict__ b,
                                                              char* __restrict__ out) {
    __shared__ __attribute__((aligned(256))) char lds[163840];
    const unsigned L = (blockIdx.x & 7u) * 32u + (blockIdx.x >> 3);
    if (L >= 252u) return;
    const unsigned rg = L / 7u, cg = L - rg * 7u;
    const unsigned wv = __builtin_amdgcn_readfirstlane(threadIdx.x >> 6);
    const unsigned p0 = (cg + 3u * rg) % 7u;
    const uint64_t xmask = 0x33198cc63ull;
    const unsigned extra = (unsigned)(xmask >> rg) & 1u;
    const unsigned b0 = rg * 28u + (unsigned)__builtin_popcountll(xmask & ((1ull << rg) - 1ull));
    const unsigned nld = 13u + (extra & (1u - (wv >> 1)));
    const unsigned woff = cg * 128u + wv * 7168u;
    const uint64_t wp = (uint64_t)w + woff;
    const unsigned xoff = (b0 + (wv >> 1)) * 14336u + (wv & 1u) * 7168u;
    const uint64_t xp = (uint64_t)x + xoff;
    const unsigned xend = (b0 + 28u + extra) * 14336u;
    const unsigned ooff = b0 * 14336u + cg * 128u;
    const uint64_t op = (uint64_t)out + ooff;
    u32x4 wsrd = {(unsigned)wp, (unsigned)(wp >> 32) & 0xffffu, 1204224u - woff, 0x20000u};
    u32x4 osrd = {(unsigned)op, (unsigned)(op >> 32) & 0xffffu, xend - ooff, 0x20000u};
    const unsigned xs0 = (unsigned)xp, xs1 = (unsigned)(xp >> 32) & 0xffffu, xs2 = xend - xoff;
    const unsigned coff = (cg * 64u + (wv & 1u) * 32u) * 2u;
    const char* pg = g + coff; const char* pvar = var + coff; const char* pmean = mean + coff; const char* pb = b + coff;
    const unsigned ci = L & 31u;
    unsigned tid = threadIdx.x;

    asm volatile(
        "s_getpc_b64 s[68:69]\n\t"
        "CPF%=:\n\t"
        "s_lshl_b32 s63, %[ci], 2\n\t"
        "s_add_u32 s63, s63, %[wv]\n\t"
        "s_lshl_b32 s63, s63, 8\n\t"
        "v_and_b32 v22, 1, v0\n\t"
        "v_lshlrev_b32 v22, 7, v22\n\t"
        "v_add_u32 v22, s63, v22\n\t"
        "s_and_b32 s69, s69, 0xffff\n\t"
        "s_mov_b32 s70, END%=-CPF%=\n\t"
        "s_mov_b32 s71, 0x20000\n\t"
        "buffer_load_dword v23, v22, s[68:71], 0 offen\n\t"
        "v_and_b32 v17, 63, v0\n\t"
        "s_mul_i32 s51, %[p0], 0xe000\n\t"
        "v_lshrrev_b32 v18, 4, v17\n\t"
        "v_lshlrev_b32 v19, 4, v18\n\t"
        "global_load_dwordx4 v[132:135], v19, %[pvar]\n\t"
        "global_load_dwordx4 v[128:131], v19, %[pg]\n\t"
        "global_load_dwordx4 v[136:139], v19, %[pmean]\n\t"
        "global_load_dwordx4 v[140:143], v19, %[pb]\n\t"
        "v_and_b32 v24, 7, v17\n\t"
        "v_lshrrev_b32 v25, 3, v17\n\t"
        "s_mul_i32 s41, %[wv], 0x408\n\t"
        "v_mul_u32_u24 v25, 0x380, v25\n\t"
        "v_and_b32 v8, 1, v18\n\t"
        "s_add_u32 s41, s41, 0x1000\n\t"
        "v_lshlrev_b32 v8, 2, v8\n\t"
        "v_xor_b32 v8, v8, v24\n\t"
        "s_lshl_b32 s40, %[wv], 10\n\t"
        "v_lshl_add_u32 v8, v8, 4, v25\n\t"
        "s_and_b32 s66, %[wv], 1\n\t"
        "s_lshr_b32 s67, %[wv], 1\n\t"
        "s_lshl_b32 s63, s66, 2\n\t"
        "v_add_u32 v14, s63, v18\n\t"
        "v_xor_b32 v14, v14, v24\n\t"
        "v_lshl_add_u32 v14, v14, 4, v25\n\t"
        "s_lshl_b32 s63, %[p0], 7\n\t"
        "s_mov_b32 s36, %[xs0]\n\t"
        "s_mov_b32 s37, %[xs1]\n\t"
        "s_mov_b32 s38, %[xs2]\n\t"
        "s_mov_b32 s39, 0x20000\n\t"
        "v_add_u32 v1, s63, v14\n\t"
        "s_mov_b32 s42, 0\n\t"
        "s_mov_b32 s43, 0x7000\n\t"
        "s_mov_b32 s44, 0x62000\n\t"
        "s_mov_b32 s45, 0x69000\n\t"
        "s_mov_b32 s46, 0xc4000\n\t"
        "s_mov_b32 s47, 0xcb000\n\t"
        "v_add_u32 v9, s51, v8\n\t"
        "s_add_u32 s51, s51, 0xe000\n\t"
        "s_cmp_eq_u32 s51, 0x62000\n\t"
        "s_cselect_b32 s51, 0, s51\n\t"
        "s_add_u32 m0, s41, 0xe200\n\t"
        "s_nop 0\n\t"
        "buffer_load_dwordx4 v9, %[wsrd], s42 offen lds\n\t"
        "s_add_u32 m0, s40, 0xe200\n\t"
        "s_nop 0\n\t"
        "buffer_load_dwordx4 v1, s[36:39], 0 offen lds\n\t"
        "s_add_u32 m0, s41, 0x10240\n\t"
        "s_nop 0\n\t"
        "buffer_load_dwordx4 v9, %[wsrd], s44 offen lds\n\t"
        "s_add_u32 m0, s41, 0x12280\n\t"
        "s_nop 0\n\t"
        "buffer_load_dwordx4 v9, %[wsrd], s46 offen lds\n\t"
        "s_add_u32 m0, s41, 0xf220\n\t"
        "s_nop 0\n\t"
        "buffer_load_dwordx4 v9, %[wsrd], s43 offen lds\n\t"
        "s_add_u32 m0, s41, 0x11260\n\t"
        "s_nop 0\n\t"
        "buffer_load_dwordx4 v9, %[wsrd], s45 offen lds\n\t"
        "s_add_u32 m0, s41, 0x132a0\n\t"
        "s_nop 0\n\t"
        "buffer_load_dwordx4 v9, %[wsrd], s47 offen lds\n\t"
        "s_lshl_b32 s63, %[p0], 7\n\t"
        "s_add_u32 s63, s63, 128\n\t"
        "s_cmp_eq_u32 s63, 0x380\n\t"
        "s_cselect_b32 s63, 0, s63\n\t"
        "v_add_u32 v2, s63, v14\n\t"
        "s_add_u32 s63, s63, 128\n\t"
        "s_cmp_eq_u32 s63, 0x380\n\t"
        "s_cselect_b32 s63, 0, s63\n\t"
        "v_add_u32 v3, s63, v14\n\t"
        "s_add_u32 s63, s63, 128\n\t"
        "s_cmp_eq_u32 s63, 0x380\n\t"
        "s_cselect_b32 s63, 0, s63\n\t"
        "v_add_u32 v4, s63, v14\n\t"
        "s_add_u32 s63, s63, 128\n\t"
        "s_cmp_eq_u32 s63, 0x380\n\t"
        "s_cselect_b32 s63, 0, s63\n\t"
        "v_add_u32 v5, s63, v14\n\t"
        "s_add_u32 s63, s63, 128\n\t"
        "s_cmp_eq_u32 s63, 0x380\n\t"
        "s_cselect_b32 s63, 0, s63\n\t"
        "v_add_u32 v6, s63, v14\n\t"
        "s_add_u32 s63, s63, 128\n\t"
        "s_cmp_eq_u32 s63, 0x380\n\t"
        "s_cselect_b32 s63, 0, s63\n\t"
        "v_add_u32 v7, s63, v14\n\t"
        "s_mul_i32 s58, s67, 0x3800\n\t"
        "s_lshl_b32 s64, s67, 11\n\t"
        "s_lshl_b32 s63, s66, 10\n\t"
        "s_add_u32 s60, s58, s63\n\t"
        "s_mov_b32 s59, s38\n\t"
        "s_mov_b32 s52, 0\n\t"
        "s_mov_b32 s62, -1\n\t"
        "s_mov_b32 s53, s58\n\t"
        "s_add_u32 s52, s52, 0x7000\n\t"
        "s_add_u32 s62, s62, 1\n\t"
        "s_cmp_lt_u32 s62, %[nld]\n\t"
        "s_cselect_b32 s38, s59, 0\n\t"
        "s_add_u32 s56, s60, 0x15300\n\t"
        "s_mov_b32 m0, s56\n\t"
        "s_nop 0\n\t"
        "buffer_load_dwordx4 v1, s[36:39], s52 offen lds\n\t"
        "s_add_u32 m0, s56, 0x800\n\t"
        "s_nop 0\n\t"
        "buffer_load_dwordx4 v2, s[36:39], s52 offen lds\n\t"
        "s_add_u32 m0, s56, 0x1000\n\t"
        "s_nop 0\n\t"
        "buffer_load_dwordx4 v3, s[36:39], s52 offen lds\n\t"
        "s_add_u32 m0, s56, 0x1800\n\t"
        "s_nop 0\n\t"
        "buffer_load_dwordx4 v4, s[36:39], s52 offen lds\n\t"
        "s_add_u32 m0, s56, 0x2000\n\t"
        "s_nop 0\n\t"
        "buffer_load_dwordx4 v5, s[36:39], s52 offen lds\n\t"
        "s_add_u32 m0, s56, 0x2800\n\t"
        "s_nop 0\n\t"
        "buffer_load_dwordx4 v6, s[36:39], s52 offen lds\n\t"
        "s_add_u32 m0, s56, 0x3000\n\t"
        "s_nop 0\n\t"
        "buffer_load_dwordx4 v7, s[36:39], s52 offen lds\n\t"
        "v_and_b32 v26, 15, v17\n\t"
        "v_and_b32 v10, 3, v17\n\t"
        "s_lshl_b32 s63, s66, 2\n\t"
        "v_add_u32 v10, s63, v10\n\t"
        "v_bfe_u32 v19, v17, 3, 1\n\t"
        "v_lshlrev_b32 v19, 2, v19\n\t"
        "v_xor_b32 v10, v10, v19\n\t"
        "v_lshlrev_b32 v10, 4, v10\n\t"
        "v_bfe_u32 v19, v17, 2, 2\n\t"
        "v_lshl_add_u32 v10, v19, 7, v10\n\t"
        "v_mul_u32_u24 v19, 0x408, v18\n\t"
        "v_add_u32 v10, v10, v19\n\t"
        "v_lshrrev_b32 v12, 1, v26\n\t"
        "v_xor_b32 v12, v12, v18\n\t"
        "v_lshlrev_b32 v12, 4, v12\n\t"
        "v_and_b32 v19, 7, v26\n\t"
        "v_lshl_add_u32 v12, v19, 7, v12\n\t"
        "v_lshrrev_b32 v19, 3, v26\n\t"
        "v_lshl_add_u32 v12, v19, 10, v12\n\t"
        "v_mul_u32_u24 v16, 0x380, v26\n\t"
        "v_lshl_add_u32 v16, v18, 4, v16\n\t"
        "s_lshl_b32 s63, s66, 6\n\t"
        "v_add_u32 v16, s63, v16\n\t"
        "s_lshl_b32 s63, s66, 2\n\t"
        "v_add_u32 v164, s63, v18\n\t"
        "v_and_b32 v19, 7, v26\n\t"
        "v_xor_b32 v164, v164, v19\n\t"
        "v_lshlrev_b32 v164, 4, v164\n\t"
        "v_lshl_add_u32 v164, v26, 7, v164\n\t"
        "v_add_u32 v164, s64, v164\n\t"
        "v_lshrrev_b32 v19, 3, v17\n\t"
        "v_and_b32 v165, 7, v17\n\t"
        "v_xor_b32 v165, v165, v19\n\t"
        "v_lshlrev_b32 v165, 4, v165\n\t"
        "v_lshl_add_u32 v165, v19, 7, v165\n\t"
        "s_lshl_b32 s63, s66, 10\n\t"
        "s_add_u32 s63, s63, s64\n\t"
        "v_add_u32 v165, s63, v165\n\t"
        "v_mul_u32_u24 v13, 0x380, v19\n\t"
        "v_and_b32 v19, 7, v17\n\t"
        "v_lshl_add_u32 v13, v19, 4, v13\n\t"
        "s_mul_i32 s63, s66, 0x1c00\n\t"
        "v_add_u32 v13, s63, v13\n\t"
        "s_mov_b32 s61, 0xbfb8aa3b\n\t"
        "s_waitcnt vmcnt(14)\n\t"
        "v_lshlrev_b32 v32, 16, v132\n\t"
        "v_add_f32 v32, 0x3727c5ac, v32\n\t"
        "v_rsq_f32 v32, v32\n\t"
        "v_lshlrev_b32 v19, 16, v128\n\t"
        "v_mul_f32 v19, s61, v19\n\t"
        "v_mul_f32 v32, v32, v19\n\t"
        "v_lshlrev_b32 v40, 16, v140\n\t"
        "v_mul_f32 v40, s61, v40\n\t"
        "v_lshlrev_b32 v19, 16, v136\n\t"
        "v_fma_f32 v40, -v19, v32, v40\n\t"
        "v_and_b32 v33, 0xffff0000, v132\n\t"
        "v_add_f32 v33, 0x3727c5ac, v33\n\t"
        "v_rsq_f32 v33, v33\n\t"
        "v_and_b32 v19, 0xffff0000, v128\n\t"
        "v_mul_f32 v19, s61, v19\n\t"
        "v_mul_f32 v33, v33, v19\n\t"
        "v_and_b32 v41, 0xffff0000, v140\n\t"
        "v_mul_f32 v41, s61, v41\n\t"
        "v_and_b32 v19, 0xffff0000, v136\n\t"
        "v_fma_f32 v41, -v19, v33, v41\n\t"
        "v_lshlrev_b32 v34, 16, v133\n\t"
        "v_add_f32 v34, 0x3727c5ac, v34\n\t"
        "v_rsq_f32 v34, v34\n\t"
        "v_lshlrev_b32 v19, 16, v129\n\t"
        "v_mul_f32 v19, s61, v19\n\t"
        "v_mul_f32 v34, v34, v19\n\t"
        "v_lshlrev_b32 v42, 16, v141\n\t"
        "v_mul_f32 v42, s61, v42\n\t"
        "v_lshlrev_b32 v19, 16, v137\n\t"
        "v_fma_f32 v42, -v19, v34, v42\n\t"
        "v_and_b32 v35, 0xffff0000, v133\n\t"
        "v_add_f32 v35, 0x3727c5ac, v35\n\t"
        "v_rsq_f32 v35, v35\n\t"
        "v_and_b32 v19, 0xffff0000, v129\n\t"
        "v_mul_f32 v19, s61, v19\n\t"
        "v_mul_f32 v35, v35, v19\n\t"
        "v_and_b32 v43, 0xffff0000, v141\n\t"
        "v_mul_f32 v43, s61, v43\n\t"
        "v_and_b32 v19, 0xffff0000, v137\n\t"
        "v_fma_f32 v43, -v19, v35, v43\n\t"
        "v_lshlrev_b32 v36, 16, v134\n\t"
        "v_add_f32 v36, 0x3727c5ac, v36\n\t"
        "v_rsq_f32 v36, v36\n\t"
        "v_lshlrev_b32 v19, 16, v130\n\t"
        "v_mul_f32 v19, s61, v19\n\t"
        "v_mul_f32 v36, v36, v19\n\t"
        "v_lshlrev_b32 v44, 16, v142\n\t"
        "v_mul_f32 v44, s61, v44\n\t"
        "v_lshlrev_b32 v19, 16, v138\n\t"
        "v_fma_f32 v44, -v19, v36, v44\n\t"
        "v_and_b32 v37, 0xffff0000, v134\n\t"
        "v_add_f32 v37, 0x3727c5ac, v37\n\t"
        "v_rsq_f32 v37, v37\n\t"
        "v_and_b32 v19, 0xffff0000, v130\n\t"
        "v_mul_f32 v19, s61, v19\n\t"
        "v_mul_f32 v37, v37, v19\n\t"
        "v_and_b32 v45, 0xffff0000, v142\n\t"
        "v_mul_f32 v45, s61, v45\n\t"
        "v_and_b32 v19, 0xffff0000, v138\n\t"
        "v_fma_f32 v45, -v19, v37, v45\n\t"
        "v_lshlrev_b32 v38, 16, v135\n\t"
        "v_add_f32 v38, 0x3727c5ac, v38\n\t"
        "v_rsq_f32 v38, v38\n\t"
        "v_lshlrev_b32 v19, 16, v131\n\t"
        "v_mul_f32 v19, s61, v19\n\t"
        "v_mul_f32 v38, v38, v19\n\t"
        "v_lshlrev_b32 v46, 16, v143\n\t"
        "v_mul_f32 v46, s61, v46\n\t"
        "v_lshlrev_b32 v19, 16, v139\n\t"
        "v_fma_f32 v46, -v19, v38, v46\n\t"
        "v_and_b32 v39, 0xffff0000, v135\n\t"
        "v_add_f32 v39, 0x3727c5ac, v39\n\t"
        "v_rsq_f32 v39, v39\n\t"
        "v_and_b32 v19, 0xffff0000, v131\n\t"
        "v_mul_f32 v19, s61, v19\n\t"
        "v_mul_f32 v39, v39, v19\n\t"
        "v_and_b32 v47, 0xffff0000, v143\n\t"
        "v_mul_f32 v47, s61, v47\n\t"
        "v_and_b32 v19, 0xffff0000, v139\n\t"
        "v_fma_f32 v47, -v19, v39, v47\n\t"
        "s_add_u32 s63, s64, 0xe200\n\t"
        "v_add_u32 v11, 0xf200, v10\n\t"
        "v_add_u32 v14, s63, v12\n\t"
        "v_xor_b32 v15, 64, v14\n\t"
        "s_waitcnt vmcnt(7)\n\t"
        "s_barrier\n\t"
        "ds_read_b128 v[152:155], v14\n\t"
        "ds_read_b64_tr_b16 a[0:1], v11\n\t"
        "ds_read_b64_tr_b16 a[2:3], v11 offset:512\n\t"
        "ds_read_b128 v[156:159], v15\n\t"
        "ds_read_b64_tr_b16 a[4:5], v11 offset:4128\n\t"
        "ds_read_b64_tr_b16 a[6:7], v11 offset:4640\n\t"
        "ds_read_b64_tr_b16 a[56:57], v11 offset:8256\n\t"
        "ds_read_b64_tr_b16 a[58:59], v11 offset:8768\n\t"
        "v_add_u32 v9, s51, v8\n\t"
        "s_add_u32 s51, s51, 0xe000\n\t"
        "s_cmp_eq_u32 s51, 0x62000\n\t"
        "s_cselect_b32 s51, 0, s51\n\t"
        "s_add_u32 m0, s41, 0\n\t"
        "s_nop 0\n\t"
        "buffer_load_dwordx4 v9, %[wsrd], s42 offen lds\n\t"
        "s_add_u32 m0, s40, 0\n\t"
        "s_nop 0\n\t"
        "buffer_load_dwordx4 v2, s[36:39], 0 offen lds\n\t"
        "s_add_u32 m0, s41, 0x2040\n\t"
        "s_nop 0\n\t"
        "buffer_load_dwordx4 v9, %[wsrd], s44 offen lds\n\t"
        "s_add_u32 m0, s41, 0x4080\n\t"
        "s_nop 0\n\t"
        "buffer_load_dwordx4 v9, %[wsrd], s46 offen lds\n\t"
        "s_add_u32 m0, s41, 0x1020\n\t"
        "s_nop 0\n\t"
        "buffer_load_dwordx4 v9, %[wsrd], s43 offen lds\n\t"
        "s_add_u32 m0, s41, 0x3060\n\t"
        "s_nop 0\n\t"
        "buffer_load_dwordx4 v9, %[wsrd], s45 offen lds\n\t"
        "s_add_u32 m0, s41, 0x50a0\n\t"
        "s_nop 0\n\t"
        "buffer_load_dwordx4 v9, %[wsrd], s47 offen lds\n\t"
        "s_waitcnt lgkmcnt(5)\n\t"
        "v_mfma_f32_16x16x32_bf16 v[128:131], a[0:3], v[152:155], 0\n\t"
        "ds_read_b64_tr_b16 a[60:61], v11 offset:12384\n\t"
        "ds_read_b64_tr_b16 a[62:63], v11 offset:12896\n\t"
        "v_add_u32 v9, s51, v8\n\t"
        "s_add_u32 s51, s51, 0xe000\n\t"
        "s_cmp_eq_u32 s51, 0x62000\n\t"
        "s_cselect_b32 s51, 0, s51\n\t"
        "s_waitcnt lgkmcnt(4)\n\t"
        "v_mfma_f32_16x16x32_bf16 v[128:131], a[4:7], v[156:159], v[128:131]\n\t"
        "ds_read_b64_tr_b16 a[112:113], v11 offset:16512\n\t"
        "ds_read_b64_tr_b16 a[114:115], v11 offset:17024\n\t"
        "s_add_u32 m0, s41, 0x7100\n\t"
        "s_nop 0\n\t"
        "buffer_load_dwordx4 v9, %[wsrd], s42 offen lds\n\t"
        "s_waitcnt lgkmcnt(4)\n\t"
        "v_mfma_f32_16x16x32_bf16 v[132:135], a[56:59], v[152:155], 0\n\t"
        "s_waitcnt lgkmcnt(2)\n\t"
        "ds_read_b64_tr_b16 a[116:117], v11 offset:20640\n\t"
        "ds_read_b64_tr_b16 a[118:119], v11 offset:21152\n\t"
        "v_mfma_f32_16x16x32_bf16 v[132:135], a[60:63], v[156:159], v[132:135]\n\t"
        "ds_read_b64_tr_b16 a[168:169], v11 offset:8\n\t"
        "ds_read_b64_tr_b16 a[170:171], v11 offset:520\n\t"
        "s_add_u32 m0, s40, 0x7100\n\t"
        "s_nop 0\n\t"
        "buffer_load_dwordx4 v3, s[36:39], 0 offen lds\n\t"
        "s_waitcnt lgkmcnt(4)\n\t"
        "v_mfma_f32_16x16x32_bf16 v[136:139], a[112:115], v[152:155], 0\n\t"
        "ds_read_b64_tr_b16 a[172:173], v11 offset:4136\n\t"
        "ds_read_b64_tr_b16 a[174:175], v11 offset:4648\n\t"
        "s_add_u32 m0, s41, 0x9140\n\t"
        "s_nop 0\n\t"
        "buffer_load_dwordx4 v9, %[wsrd], s44 offen lds\n\t"
        "s_waitcnt lgkmcnt(4)\n\t"
        "v_mfma_f32_16x16x32_bf16 v[136:139], a[116:119], v[156:159], v[136:139]\n\t"
        "s_waitcnt lgkmcnt(2)\n\t"
        "ds_read_b64_tr_b16 a[224:225], v11 offset:8264\n\t"
        "ds_read_b64_tr_b16 a[226:227], v11 offset:8776\n\t"
        "v_mfma_f32_16x16x32_bf16 v[140:143], a[168:171], v[152:155], 0\n\t"
        "ds_read_b64_tr_b16 a[228:229], v11 offset:12392\n\t"
        "ds_read_b64_tr_b16 a[230:231], v11 offset:12904\n\t"
        "s_add_u32 m0, s41, 0xb180\n\t"
        "s_nop 0\n\t"
        "buffer_load_dwordx4 v9, %[wsrd], s46 offen lds\n\t"
        "s_waitcnt lgkmcnt(4)\n\t"
        "v_mfma_f32_16x16x32_bf16 v[140:143], a[172:175], v[156:159], v[140:143]\n\t"
        "ds_read_b64_tr_b16 v[200:201], v11 offset:16520\n\t"
        "ds_read_b64_tr_b16 v[202:203], v11 offset:17032\n\t"
        "s_add_u32 m0, s41, 0x8120\n\t"
        "s_nop 0\n\t"
        "buffer_load_dwordx4 v9, %[wsrd], s43 offen lds\n\t"
        "s_waitcnt lgkmcnt(4)\n\t"
        "v_mfma_f32_16x16x32_bf16 v[144:147], a[224:227], v[152:155], 0\n\t"
        "s_waitcnt lgkmcnt(2)\n\t"
        "ds_read_b64_tr_b16 v[204:205], v11 offset:20648\n\t"
        "ds_read_b64_tr_b16 v[206:207], v11 offset:21160\n\t"
        "v_mfma_f32_16x16x32_bf16 v[144:147], a[228:231], v[156:159], v[144:147]\n\t"
        "s_add_u32 m0, s41, 0xa160\n\t"
        "s_nop 0\n\t"
        "buffer_load_dwordx4 v9, %[wsrd], s45 offen lds\n\t"
        "s_waitcnt lgkmcnt(2)\n\t"
        "v_mfma_f32_16x16x32_bf16 v[148:151], v[200:203], v[152:155], 0\n\t"
        "s_add_u32 m0, s41, 0xc1a0\n\t"
        "s_nop 0\n\t"
        "buffer_load_dwordx4 v9, %[wsrd], s47 offen lds\n\t"
        "s_waitcnt lgkmcnt(0)\n\t"
        "v_mfma_f32_16x16x32_bf16 v[148:151], v[204:207], v[156:159], v[148:151]\n\t"
        "s_add_u32 s63, s64, 0\n\t"
        "s_waitcnt lgkmcnt(0)\n\t"
        "v_add_u32 v11, 0x1000, v10\n\t"
        "v_add_u32 v14, s63, v12\n\t"
        "v_xor_b32 v15, 64, v14\n\t"
        "s_waitcnt vmcnt(7)\n\t"
        "s_barrier\n\t"
        "ds_read_b128 v[152:155], v14\n\t"
        "ds_read_b64_tr_b16 a[8:9], v11\n\t"
        "ds_read_b64_tr_b16 a[10:11], v11 offset:512\n\t"
        "ds_read_b128 v[156:159], v15\n\t"
        "ds_read_b64_tr_b16 a[12:13], v11 offset:4128\n\t"
        "ds_read_b64_tr_b16 a[14:15], v11 offset:4640\n\t"
        "ds_read_b64_tr_b16 a[64:65], v11 offset:8256\n\t"
        "ds_read_b64_tr_b16 a[66:67], v11 offset:8768\n\t"
        "s_waitcnt lgkmcnt(5)\n\t"
        "v_mfma_f32_16x16x32_bf16 v[128:131], a[8:11], v[152:155], v[128:131]\n\t"
        "ds_read_b64_tr_b16 a[68:69], v11 offset:12384\n\t"
        "ds_read_b64_tr_b16 a[70:71], v11 offset:12896\n\t"
        "v_add_u32 v9, s51, v8\n\t"
        "s_add_u32 s51, s51, 0xe000\n\t"
        "s_cmp_eq_u32 s51, 0x62000\n\t"
        "s_cselect_b32 s51, 0, s51\n\t"
        "s_waitcnt lgkmcnt(4)\n\t"
        "v_mfma_f32_16x16x32_bf16 v[128:131], a[12:15], v[156:159], v[128:131]\n\t"
        "ds_read_b64_tr_b16 a[120:121], v11 offset:16512\n\t"
        "ds_read_b64_tr_b16 a[122:123], v11 offset:17024\n\t"
        "s_add_u32 m0, s41, 0xe200\n\t"
        "s_nop 0\n\t"
        "buffer_load_dwordx4 v9, %[wsrd], s42 offen lds\n\t"
        "s_waitcnt lgkmcnt(4)\n\t"
        "v_mfma_f32_16x16x32_bf16 v[132:135], a[64:67], v[152:155], v[132:135]\n\t"
        "s_waitcnt lgkmcnt(2)\n\t"
        "ds_read_b64_tr_b16 a[124:125], v11 offset:20640\n\t"
        "ds_read_b64_tr_b16 a[126:127], v11 offset:21152\n\t"
        "v_mfma_f32_16x16x32_bf16 v[132:135], a[68:71], v[156:159], v[132:135]\n\t"
        "ds_read_b64_tr_b16 a[176:177], v11 offset:8\n\t"
        "ds_read_b64_tr_b16 a[178:179], v11 offset:520\n\t"
        "s_add_u32 m0, s40, 0xe200\n\t"
        "s_nop 0\n\t"
        "buffer_load_dwordx4 v4, s[36:39], 0 offen lds\n\t"
        "s_waitcnt lgkmcnt(4)\n\t"
        "v_mfma_f32_16x16x32_bf16 v[136:139], a[120:123], v[152:155], v[136:139]\n\t"
        "ds_read_b64_tr_b16 a[180:181], v11 offset:4136\n\t"
        "ds_read_b64_tr_b16 a[182:183], v11 offset:4648\n\t"
        "s_add_u32 m0, s41, 0x10240\n\t"
        "s_nop 0\n\t"
        "buffer_load_dwordx4 v9, %[wsrd], s44 offen lds\n\t"
        "s_waitcnt lgkmcnt(4)\n\t"
        "v_mfma_f32_16x16x32_bf16 v[136:139], a[124:127], v[156:159], v[136:139]\n\t"
        "s_waitcnt lgkmcnt(2)\n\t"
        "ds_read_b64_tr_b16 a[232:233], v11 offset:8264\n\t"
        "ds_read_b64_tr_b16 a[234:235], v11 offset:8776\n\t"
        "v_mfma_f32_16x16x32_bf16 v[140:143], a[176:179], v[152:155], v[140:143]\n\t"
        "ds_read_b64_tr_b16 a[236:237], v11 offset:12392\n\t"
        "ds_read_b64_tr_b16 a[238:239], v11 offset:12904\n\t"
        "s_add_u32 m0, s41, 0x12280\n\t"
        "s_nop 0\n\t"
        "buffer_load_dwordx4 v9, %[wsrd], s46 offen lds\n\t"
        "s_waitcnt lgkmcnt(4)\n\t"
        "v_mfma_f32_16x16x32_bf16 v[140:143], a[180:183], v[156:159], v[140:143]\n\t"
        "ds_read_b64_tr_b16 v[208:209], v11 offset:16520\n\t"
        "ds_read_b64_tr_b16 v[210:211], v11 offset:17032\n\t"
        "s_add_u32 m0, s41, 0xf220\n\t"
        "s_nop 0\n\t"
        "buffer_load_dwordx4 v9, %[wsrd], s43 offen lds\n\t"
        "s_waitcnt lgkmcnt(4)\n\t"
        "v_mfma_f32_16x16x32_bf16 v[144:147], a[232:235], v[152:155], v[144:147]\n\t"
        "s_waitcnt lgkmcnt(2)\n\t"
        "ds_read_b64_tr_b16 v[212:213], v11 offset:20648\n\t"
        "ds_read_b64_tr_b16 v[214:215], v11 offset:21160\n\t"
        "v_mfma_f32_16x16x32_bf16 v[144:147], a[236:239], v[156:159], v[144:147]\n\t"
        "s_add_u32 m0, s41, 0x11260\n\t"
        "s_nop 0\n\t"
        "buffer_load_dwordx4 v9, %[wsrd], s45 offen lds\n\t"
        "s_waitcnt lgkmcnt(2)\n\t"
        "v_mfma_f32_16x16x32_bf16 v[148:151], v[208:211], v[152:155], v[148:151]\n\t"
        "s_add_u32 m0, s41, 0x132a0\n\t"
        "s_nop 0\n\t"
        "buffer_load_dwordx4 v9, %[wsrd], s47 offen lds\n\t"
        "s_waitcnt lgkmcnt(0)\n\t"
        "v_mfma_f32_16x16x32_bf16 v[148:151], v[212:215], v[156:159], v[148:151]\n\t"
        "s_add_u32 s63, s64, 0x7100\n\t"
        "s_waitcnt lgkmcnt(0)\n\t"
        "v_add_u32 v11, 0x8100, v10\n\t"
        "v_add_u32 v14, s63, v12\n\t"
        "v_xor_b32 v15, 64, v14\n\t"
        "s_waitcnt vmcnt(7)\n\t"
        "s_barrier\n\t"
        "ds_read_b128 v[152:155], v14\n\t"
        "ds_read_b64_tr_b16 a[16:17], v11\n\t"
        "ds_read_b64_tr_b16 a[18:19], v11 offset:512\n\t"
        "ds_read_b128 v[156:159], v15\n\t"
        "ds_read_b64_tr_b16 a[20:21], v11 offset:4128\n\t"
        "ds_read_b64_tr_b16 a[22:23], v11 offset:4640\n\t"
        "ds_read_b64_tr_b16 a[72:73], v11 offset:8256\n\t"
        "ds_read_b64_tr_b16 a[74:75], v11 offset:8768\n\t"
        "s_waitcnt lgkmcnt(5)\n\t"
        "v_mfma_f32_16x16x32_bf16 v[128:131], a[16:19], v[152:155], v[128:131]\n\t"
        "ds_read_b64_tr_b16 a[76:77], v11 offset:12384\n\t"
        "ds_read_b64_tr_b16 a[78:79], v11 offset:12896\n\t"
        "v_add_u32 v9, s51, v8\n\t"
        "s_add_u32 s51, s51, 0xe000\n\t"
        "s_cmp_eq_u32 s51, 0x62000\n\t"
        "s_cselect_b32 s51, 0, s51\n\t"
        "s_waitcnt lgkmcnt(4)\n\t"
        "v_mfma_f32_16x16x32_bf16 v[128:131], a[20:23], v[156:159], v[128:131]\n\t"
        "ds_read_b64_tr_b16 a[128:129], v11 offset:16512\n\t"
        "ds_read_b64_tr_b16 a[130:131], v11 offset:17024\n\t"
        "s_add_u32 m0, s41, 0\n\t"
        "s_nop 0\n\t"
        "buffer_load_dwordx4 v9, %[wsrd], s42 offen lds\n\t"
        "s_waitcnt lgkmcnt(4)\n\t"
        "v_mfma_f32_16x16x32_bf16 v[132:135], a[72:75], v[152:155], v[132:135]\n\t"
        "s_waitcnt lgkmcnt(2)\n\t"
        "ds_read_b64_tr_b16 a[132:133], v11 offset:20640\n\t"
        "ds_read_b64_tr_b16 a[134:135], v11 offset:21152\n\t"
        "v_mfma_f32_16x16x32_bf16 v[132:135], a[76:79], v[156:159], v[132:135]\n\t"
        "ds_read_b64_tr_b16 a[184:185], v11 offset:8\n\t"
        "ds_read_b64_tr_b16 a[186:187], v11 offset:520\n\t"
        "s_add_u32 m0, s40, 0\n\t"
        "s_nop 0\n\t"
        "buffer_load_dwordx4 v5, s[36:39], 0 offen lds\n\t"
        "s_waitcnt lgkmcnt(4)\n\t"
        "v_mfma_f32_16x16x32_bf16 v[136:139], a[128:131], v[152:155], v[136:139]\n\t"
        "ds_read_b64_tr_b16 a[188:189], v11 offset:4136\n\t"
        "ds_read_b64_tr_b16 a[190:191], v11 offset:4648\n\t"
        "s_add_u32 m0, s41, 0x2040\n\t"
        "s_nop 0\n\t"
        "buffer_load_dwordx4 v9, %[wsrd], s44 offen lds\n\t"
        "s_waitcnt lgkmcnt(4)\n\t"
        "v_mfma_f32_16x16x32_bf16 v[136:139], a[132:135], v[156:159], v[136:139]\n\t"
        "s_waitcnt lgkmcnt(2)\n\t"
        "ds_read_b64_tr_b16 a[240:241], v11 offset:8264\n\t"
        "ds_read_b64_tr_b16 a[242:243], v11 offset:8776\n\t"
        "v_mfma_f32_16x16x32_bf16 v[140:143], a[184:187], v[152:155], v[140:143]\n\t"
        "ds_read_b64_tr_b16 a[244:245], v11 offset:12392\n\t"
        "ds_read_b64_tr_b16 a[246:247], v11 offset:12904\n\t"
        "s_add_u32 m0, s41, 0x4080\n\t"
        "s_nop 0\n\t"
        "buffer_load_dwordx4 v9, %[wsrd], s46 offen lds\n\t"
        "s_waitcnt lgkmcnt(4)\n\t"
        "v_mfma_f32_16x16x32_bf16 v[140:143], a[188:191], v[156:159], v[140:143]\n\t"
        "ds_read_b64_tr_b16 v[216:217], v11 offset:16520\n\t"
        "ds_read_b64_tr_b16 v[218:219], v11 offset:17032\n\t"
        "s_add_u32 m0, s41, 0x1020\n\t"
        "s_nop 0\n\t"
        "buffer_load_dwordx4 v9, %[wsrd], s43 offen lds\n\t"
        "s_waitcnt lgkmcnt(4)\n\t"
        "v_mfma_f32_16x16x32_bf16 v[144:147], a[240:243], v[152:155], v[144:147]\n\t"
        "s_waitcnt lgkmcnt(2)\n\t"
        "ds_read_b64_tr_b16 v[220:221], v11 offset:20648\n\t"
        "ds_read_b64_tr_b16 v[222:223], v11 offset:21160\n\t"
        "v_mfma_f32_16x16x32_bf16 v[144:147], a[244:247], v[156:159], v[144:147]\n\t"
        "s_add_u32 m0, s41, 0x3060\n\t"
        "s_nop 0\n\t"
        "buffer_load_dwordx4 v9, %[wsrd], s45 offen lds\n\t"
        "s_waitcnt lgkmcnt(2)\n\t"
        "v_mfma_f32_16x16x32_bf16 v[148:151], v[216:219], v[152:155], v[148:151]\n\t"
        "s_add_u32 m0, s41, 0x50a0\n\t"
        "s_nop 0\n\t"
        "buffer_load_dwordx4 v9, %[wsrd], s47 offen lds\n\t"
        "s_waitcnt lgkmcnt(0)\n\t"
        "v_mfma_f32_16x16x32_bf16 v[148:151], v[220:223], v[156:159], v[148:151]\n\t"
        "s_add_u32 s63, s64, 0xe200\n\t"
        "s_waitcnt lgkmcnt(0)\n\t"
        "v_add_u32 v11, 0xf200, v10\n\t"
        "v_add_u32 v14, s63, v12\n\t"
        "v_xor_b32 v15, 64, v14\n\t"
        "s_waitcnt vmcnt(7)\n\t"
        "s_barrier\n\t"
        "ds_read_b128 v[152:155], v14\n\t"
        "ds_read_b64_tr_b16 a[24:25], v11\n\t"
        "ds_read_b64_tr_b16 a[26:27], v11 offset:512\n\t"
        "ds_read_b128 v[156:159], v15\n\t"
        "ds_read_b64_tr_b16 a[28:29], v11 offset:4128\n\t"
        "ds_read_b64_tr_b16 a[30:31], v11 offset:4640\n\t"
        "ds_read_b64_tr_b16 a[80:81], v11 offset:8256\n\t"
        "ds_read_b64_tr_b16 a[82:83], v11 offset:8768\n\t"
        "s_waitcnt lgkmcnt(5)\n\t"
        "v_mfma_f32_16x16x32_bf16 v[128:131], a[24:27], v[152:155], v[128:131]\n\t"
        "ds_read_b64_tr_b16 a[84:85], v11 offset:12384\n\t"
        "ds_read_b64_tr_b16 a[86:87], v11 offset:12896\n\t"
        "v_add_u32 v9, s51, v8\n\t"
        "s_add_u32 s51, s51, 0xe000\n\t"
        "s_cmp_eq_u32 s51, 0x62000\n\t"
        "s_cselect_b32 s51, 0, s51\n\t"
        "s_waitcnt lgkmcnt(4)\n\t"
        "v_mfma_f32_16x16x32_bf16 v[128:131], a[28:31], v[156:159], v[128:131]\n\t"
        "ds_read_b64_tr_b16 a[136:137], v11 offset:16512\n\t"
        "ds_read_b64_tr_b16 a[138:139], v11 offset:17024\n\t"
        "s_add_u32 m0, s41, 0x7100\n\t"
        "s_nop 0\n\t"
        "buffer_load_dwordx4 v9, %[wsrd], s42 offen lds\n\t"
        "s_waitcnt lgkmcnt(4)\n\t"
        "v_mfma_f32_16x16x32_bf16 v[132:135], a[80:83], v[152:155], v[132:135]\n\t"
        "s_waitcnt lgkmcnt(2)\n\t"
        "ds_read_b64_tr_b16 a[140:141], v11 offset:20640\n\t"
        "ds_read_b64_tr_b16 a[142:143], v11 offset:21152\n\t"
        "v_mfma_f32_16x16x32_bf16 v[132:135], a[84:87], v[156:159], v[132:135]\n\t"
        "ds_read_b64_tr_b16 a[192:193], v11 offset:8\n\t"
        "ds_read_b64_tr_b16 a[194:195], v11 offset:520\n\t"
        "s_add_u32 m0, s40, 0x7100\n\t"
        "s_nop 0\n\t"
        "buffer_load_dwordx4 v6, s[36:39], 0 offen lds\n\t"
        "s_waitcnt lgkmcnt(4)\n\t"
        "v_mfma_f32_16x16x32_bf16 v[136:139], a[136:139], v[152:155], v[136:139]\n\t"
        "ds_read_b64_tr_b16 a[196:197], v11 offset:4136\n\t"
        "ds_read_b64_tr_b16 a[198:199], v11 offset:4648\n\t"
        "s_add_u32 m0, s41, 0x9140\n\t"
        "s_nop 0\n\t"
        "buffer_load_dwordx4 v9, %[wsrd], s44 offen lds\n\t"
        "s_waitcnt lgkmcnt(4)\n\t"
        "v_mfma_f32_16x16x32_bf16 v[136:139], a[140:143], v[156:159], v[136:139]\n\t"
        "s_waitcnt lgkmcnt(2)\n\t"
        "ds_read_b64_tr_b16 a[248:249], v11 offset:8264\n\t"
        "ds_read_b64_tr_b16 a[250:251], v11 offset:8776\n\t"
        "v_mfma_f32_16x16x32_bf16 v[140:143], a[192:195], v[152:155], v[140:143]\n\t"
        "ds_read_b64_tr_b16 a[252:253], v11 offset:12392\n\t"
        "ds_read_b64_tr_b16 a[254:255], v11 offset:12904\n\t"
        "s_add_u32 m0, s41, 0xb180\n\t"
        "s_nop 0\n\t"
        "buffer_load_dwordx4 v9, %[wsrd], s46 offen lds\n\t"
        "s_waitcnt lgkmcnt(4)\n\t"
        "v_mfma_f32_16x16x32_bf16 v[140:143], a[196:199], v[156:159], v[140:143]\n\t"
        "ds_read_b64_tr_b16 v[224:225], v11 offset:16520\n\t"
        "ds_read_b64_tr_b16 v[226:227], v11 offset:17032\n\t"
        "s_add_u32 m0, s41, 0x8120\n\t"
        "s_nop 0\n\t"
        "buffer_load_dwordx4 v9, %[wsrd], s43 offen lds\n\t"
        "s_waitcnt lgkmcnt(4)\n\t"
        "v_mfma_f32_16x16x32_bf16 v[144:147], a[248:251], v[152:155], v[144:147]\n\t"
        "s_waitcnt lgkmcnt(2)\n\t"
        "ds_read_b64_tr_b16 v[228:229], v11 offset:20648\n\t"
        "ds_read_b64_tr_b16 v[230:231], v11 offset:21160\n\t"
        "v_mfma_f32_16x16x32_bf16 v[144:147], a[252:255], v[156:159], v[144:147]\n\t"
        "s_add_u32 m0, s41, 0xa160\n\t"
        "s_nop 0\n\t"
        "buffer_load_dwordx4 v9, %[wsrd], s45 offen lds\n\t"
        "s_waitcnt lgkmcnt(2)\n\t"
        "v_mfma_f32_16x16x32_bf16 v[148:151], v[224:227], v[152:155], v[148:151]\n\t"
        "s_add_u32 m0, s41, 0xc1a0\n\t"
        "s_nop 0\n\t"
        "buffer_load_dwordx4 v9, %[wsrd], s47 offen lds\n\t"
        "s_waitcnt lgkmcnt(0)\n\t"
        "v_mfma_f32_16x16x32_bf16 v[148:151], v[228:231], v[156:159], v[148:151]\n\t"
        "s_add_u32 s63, s64, 0\n\t"
        "s_waitcnt lgkmcnt(0)\n\t"
        "v_add_u32 v11, 0x1000, v10\n\t"
        "v_add_u32 v14, s63, v12\n\t"
        "v_xor_b32 v15, 64, v14\n\t"
        "s_waitcnt vmcnt(7)\n\t"
        "s_barrier\n\t"
        "ds_read_b128 v[152:155], v14\n\t"
        "ds_read_b64_tr_b16 a[32:33], v11\n\t"
        "ds_read_b64_tr_b16 a[34:35], v11 offset:512\n\t"
        "ds_read_b128 v[156:159], v15\n\t"
        "ds_read_b64_tr_b16 a[36:37], v11 offset:4128\n\t"
        "ds_read_b64_tr_b16 a[38:39], v11 offset:4640\n\t"
        "ds_read_b64_tr_b16 a[88:89], v11 offset:8256\n\t"
        "ds_read_b64_tr_b16 a[90:91], v11 offset:8768\n\t"
        "s_waitcnt lgkmcnt(5)\n\t"
        "v_mfma_f32_16x16x32_bf16 v[128:131], a[32:35], v[152:155], v[128:131]\n\t"
        "ds_read_b64_tr_b16 a[92:93], v11 offset:12384\n\t"
        "ds_read_b64_tr_b16 a[94:95], v11 offset:12896\n\t"
        "v_add_u32 v9, s51, v8\n\t"
        "s_add_u32 s51, s51, 0xe000\n\t"
        "s_cmp_eq_u32 s51, 0x62000\n\t"
        "s_cselect_b32 s51, 0, s51\n\t"
        "s_waitcnt lgkmcnt(4)\n\t"
        "v_mfma_f32_16x16x32_bf16 v[128:131], a[36:39], v[156:159], v[128:131]\n\t"
        "ds_read_b64_tr_b16 a[144:145], v11 offset:16512\n\t"
        "ds_read_b64_tr_b16 a[146:147], v11 offset:17024\n\t"
        "s_add_u32 m0, s41, 0xe200\n\t"
        "s_nop 0\n\t"
        "buffer_load_dwordx4 v9, %[wsrd], s42 offen lds\n\t"
        "s_waitcnt lgkmcnt(4)\n\t"
        "v_mfma_f32_16x16x32_bf16 v[132:135], a[88:91], v[152:155], v[132:135]\n\t"
        "s_waitcnt lgkmcnt(2)\n\t"
        "ds_read_b64_tr_b16 a[148:149], v11 offset:20640\n\t"
        "ds_read_b64_tr_b16 a[150:151], v11 offset:21152\n\t"
        "v_mfma_f32_16x16x32_bf16 v[132:135], a[92:95], v[156:159], v[132:135]\n\t"
        "ds_read_b64_tr_b16 a[200:201], v11 offset:8\n\t"
        "ds_read_b64_tr_b16 a[202:203], v11 offset:520\n\t"
        "s_add_u32 m0, s40, 0xe200\n\t"
        "s_nop 0\n\t"
        "buffer_load_dwordx4 v7, s[36:39], 0 offen lds\n\t"
        "s_waitcnt lgkmcnt(4)\n\t"
        "v_mfma_f32_16x16x32_bf16 v[136:139], a[144:147], v[152:155], v[136:139]\n\t"
        "ds_read_b64_tr_b16 a[204:205], v11 offset:4136\n\t"
        "ds_read_b64_tr_b16 a[206:207], v11 offset:4648\n\t"
        "s_add_u32 m0, s41, 0x10240\n\t"
        "s_nop 0\n\t"
        "buffer_load_dwordx4 v9, %[wsrd], s44 offen lds\n\t"
        "s_waitcnt lgkmcnt(4)\n\t"
        "v_mfma_f32_16x16x32_bf16 v[136:139], a[148:151], v[156:159], v[136:139]\n\t"
        "s_waitcnt lgkmcnt(2)\n\t"
        "ds_read_b64_tr_b16 v[176:177], v11 offset:8264\n\t"
        "ds_read_b64_tr_b16 v[178:179], v11 offset:8776\n\t"
        "v_mfma_f32_16x16x32_bf16 v[140:143], a[200:203], v[152:155], v[140:143]\n\t"
        "ds_read_b64_tr_b16 v[180:181], v11 offset:12392\n\t"
        "ds_read_b64_tr_b16 v[182:183], v11 offset:12904\n\t"
        "s_add_u32 m0, s41, 0x12280\n\t"
        "s_nop 0\n\t"
        "buffer_load_dwordx4 v9, %[wsrd], s46 offen lds\n\t"
        "s_waitcnt lgkmcnt(4)\n\t"
        "v_mfma_f32_16x16x32_bf16 v[140:143], a[204:207], v[156:159], v[140:143]\n\t"
        "ds_read_b64_tr_b16 v[232:233], v11 offset:16520\n\t"
        "ds_read_b64_tr_b16 v[234:235], v11 offset:17032\n\t"
        "s_add_u32 m0, s41, 0xf220\n\t"
        "s_nop 0\n\t"
        "buffer_load_dwordx4 v9, %[wsrd], s43 offen lds\n\t"
        "s_waitcnt lgkmcnt(4)\n\t"
        "v_mfma_f32_16x16x32_bf16 v[144:147], v[176:179], v[152:155], v[144:147]\n\t"
        "s_waitcnt lgkmcnt(2)\n\t"
        "ds_read_b64_tr_b16 v[236:237], v11 offset:20648\n\t"
        "ds_read_b64_tr_b16 v[238:239], v11 offset:21160\n\t"
        "v_mfma_f32_16x16x32_bf16 v[144:147], v[180:183], v[156:159], v[144:147]\n\t"
        "s_add_u32 m0, s41, 0x11260\n\t"
        "s_nop 0\n\t"
        "buffer_load_dwordx4 v9, %[wsrd], s45 offen lds\n\t"
        "s_waitcnt lgkmcnt(2)\n\t"
        "v_mfma_f32_16x16x32_bf16 v[148:151], v[232:235], v[152:155], v[148:151]\n\t"
        "s_add_u32 m0, s41, 0x132a0\n\t"
        "s_nop 0\n\t"
        "buffer_load_dwordx4 v9, %[wsrd], s47 offen lds\n\t"
        "s_waitcnt lgkmcnt(0)\n\t"
        "v_mfma_f32_16x16x32_bf16 v[148:151], v[236:239], v[156:159], v[148:151]\n\t"
        "s_add_u32 s63, s64, 0x7100\n\t"
        "s_waitcnt lgkmcnt(0)\n\t"
        "v_add_u32 v11, 0x8100, v10\n\t"
        "v_add_u32 v14, s63, v12\n\t"
        "v_xor_b32 v15, 64, v14\n\t"
        "s_waitcnt vmcnt(7)\n\t"
        "s_barrier\n\t"
        "ds_read_b128 v[152:155], v14\n\t"
        "ds_read_b64_tr_b16 a[40:41], v11\n\t"
        "ds_read_b64_tr_b16 a[42:43], v11 offset:512\n\t"
        "ds_read_b128 v[156:159], v15\n\t"
        "ds_read_b64_tr_b16 a[44:45], v11 offset:4128\n\t"
        "ds_read_b64_tr_b16 a[46:47], v11 offset:4640\n\t"
        "ds_read_b64_tr_b16 a[96:97], v11 offset:8256\n\t"
        "ds_read_b64_tr_b16 a[98:99], v11 offset:8768\n\t"
        "s_waitcnt lgkmcnt(5)\n\t"
        "v_mfma_f32_16x16x32_bf16 v[128:131], a[40:43], v[152:155], v[128:131]\n\t"
        "ds_read_b64_tr_b16 a[100:101], v11 offset:12384\n\t"
        "ds_read_b64_tr_b16 a[102:103], v11 offset:12896\n\t"
        "s_add_u32 s52, s52, 0x7000\n\t"
        "s_add_u32 s62, s62, 1\n\t"
        "s_cmp_lt_u32 s62, %[nld]\n\t"
        "s_cselect_b32 s38, s59, 0\n\t"
        "s_add_u32 s56, s60, 0x1c400\n\t"
        "s_waitcnt lgkmcnt(4)\n\t"
        "v_mfma_f32_16x16x32_bf16 v[128:131], a[44:47], v[156:159], v[128:131]\n\t"
        "ds_read_b64_tr_b16 a[152:153], v11 offset:16512\n\t"
        "ds_read_b64_tr_b16 a[154:155], v11 offset:17024\n\t"
        "s_mov_b32 m0, s56\n\t"
        "s_nop 0\n\t"
        "buffer_load_dwordx4 v1, s[36:39], s52 offen lds\n\t"
        "s_waitcnt lgkmcnt(4)\n\t"
        "v_mfma_f32_16x16x32_bf16 v[132:135], a[96:99], v[152:155], v[132:135]\n\t"
        "s_waitcnt lgkmcnt(2)\n\t"
        "ds_read_b64_tr_b16 a[156:157], v11 offset:20640\n\t"
        "ds_read_b64_tr_b16 a[158:159], v11 offset:21152\n\t"
        "v_mfma_f32_16x16x32_bf16 v[132:135], a[100:103], v[156:159], v[132:135]\n\t"
        "ds_read_b64_tr_b16 a[208:209], v11 offset:8\n\t"
        "ds_read_b64_tr_b16 a[210:211], v11 offset:520\n\t"
        "s_add_u32 m0, s56, 0x800\n\t"
        "s_nop 0\n\t"
        "buffer_load_dwordx4 v2, s[36:39], s52 offen lds\n\t"
        "s_waitcnt lgkmcnt(4)\n\t"
        "v_mfma_f32_16x16x32_bf16 v[136:139], a[152:155], v[152:155], v[136:139]\n\t"
        "ds_read_b64_tr_b16 a[212:213], v11 offset:4136\n\t"
        "ds_read_b64_tr_b16 a[214:215], v11 offset:4648\n\t"
        "s_add_u32 m0, s56, 0x1000\n\t"
        "s_nop 0\n\t"
        "buffer_load_dwordx4 v3, s[36:39], s52 offen lds\n\t"
        "s_waitcnt lgkmcnt(4)\n\t"
        "v_mfma_f32_16x16x32_bf16 v[136:139], a[156:159], v[156:159], v[136:139]\n\t"
        "s_waitcnt lgkmcnt(2)\n\t"
        "ds_read_b64_tr_b16 v[184:185], v11 offset:8264\n\t"
        "ds_read_b64_tr_b16 v[186:187], v11 offset:8776\n\t"
        "v_mfma_f32_16x16x32_bf16 v[140:143], a[208:211], v[152:155], v[140:143]\n\t"
        "ds_read_b64_tr_b16 v[188:189], v11 offset:12392\n\t"
        "ds_read_b64_tr_b16 v[190:191], v11 offset:12904\n\t"
        "s_add_u32 m0, s56, 0x1800\n\t"
        "s_nop 0\n\t"
        "buffer_load_dwordx4 v4, s[36:39], s52 offen lds\n\t"
        "s_waitcnt lgkmcnt(4)\n\t"
        "v_mfma_f32_16x16x32_bf16 v[140:143], a[212:215], v[156:159], v[140:143]\n\t"
        "ds_read_b64_tr_b16 v[240:241], v11 offset:16520\n\t"
        "ds_read_b64_tr_b16 v[242:243], v11 offset:17032\n\t"
        "s_add_u32 m0, s56, 0x2000\n\t"
        "s_nop 0\n\t"
        "buffer_load_dwordx4 v5, s[36:39], s52 offen lds\n\t"
        "s_waitcnt lgkmcnt(4)\n\t"
        "v_mfma_f32_16x16x32_bf16 v[144:147], v[184:187], v[152:155], v[144:147]\n\t"
        "s_waitcnt lgkmcnt(2)\n\t"
        "ds_read_b64_tr_b16 v[244:245], v11 offset:20648\n\t"
        "ds_read_b64_tr_b16 v[246:247], v11 offset:21160\n\t"
        "v_mfma_f32_16x16x32_bf16 v[144:147], v[188:191], v[156:159], v[144:147]\n\t"
        "s_add_u32 m0, s56, 0x2800\n\t"
        "s_nop 0\n\t"
        "buffer_load_dwordx4 v6, s[36:39], s52 offen lds\n\t"
        "s_waitcnt lgkmcnt(2)\n\t"
        "v_mfma_f32_16x16x32_bf16 v[148:151], v[240:243], v[152:155], v[148:151]\n\t"
        "s_add_u32 m0, s56, 0x3000\n\t"
        "s_nop 0\n\t"
        "buffer_load_dwordx4 v7, s[36:39], s52 offen lds\n\t"
        "s_waitcnt lgkmcnt(0)\n\t"
        "v_mfma_f32_16x16x32_bf16 v[148:151], v[244:247], v[156:159], v[148:151]\n\t"
        "s_add_u32 s63, s58, 0x15300\n\t"
        "v_add_u32 v14, s63, v12\n\t"
        "v_xor_b32 v15, 64, v14\n\t"
        "ds_read_b128 v[48:51], v14\n\t"
        "ds_read_b128 v[52:55], v15\n\t"
        "ds_read_b128 v[56:59], v14 offset:2048\n\t"
        "ds_read_b128 v[60:63], v15 offset:2048\n\t"
        "ds_read_b128 v[64:67], v14 offset:4096\n\t"
        "ds_read_b128 v[68:71], v15 offset:4096\n\t"
        "ds_read_b128 v[72:75], v14 offset:6144\n\t"
        "ds_read_b128 v[76:79], v15 offset:6144\n\t"
        "ds_read_b128 v[80:83], v14 offset:8192\n\t"
        "ds_read_b128 v[84:87], v15 offset:8192\n\t"
        "ds_read_b128 v[88:91], v14 offset:10240\n\t"
        "ds_read_b128 v[92:95], v15 offset:10240\n\t"
        "ds_read_b128 v[96:99], v14 offset:12288\n\t"
        "ds_read_b128 v[100:103], v15 offset:12288\n\t"
        "s_add_u32 s63, s64, 0xe200\n\t"
        "s_waitcnt lgkmcnt(14)\n\t"
        "v_add_u32 v11, 0xf200, v10\n\t"
        "v_add_u32 v14, s63, v12\n\t"
        "v_xor_b32 v15, 64, v14\n\t"
        "s_waitcnt vmcnt(7)\n\t"
        "s_barrier\n\t"
        "ds_read_b128 v[152:155], v14\n\t"
        "s_waitcnt lgkmcnt(10)\n\t"
        "s_waitcnt lgkmcnt(5)\n\t"
        "ds_read_b64_tr_b16 a[48:49], v11\n\t"
        "ds_read_b64_tr_b16 a[50:51], v11 offset:512\n\t"
        "ds_read_b128 v[156:159], v15\n\t"
        "ds_read_b64_tr_b16 a[52:53], v11 offset:4128\n\t"
        "ds_read_b64_tr_b16 a[54:55], v11 offset:4640\n\t"
        "ds_read_b64_tr_b16 a[104:105], v11 offset:8256\n\t"
        "ds_read_b64_tr_b16 a[106:107], v11 offset:8768\n\t"
        "s_waitcnt lgkmcnt(5)\n\t"
        "v_mfma_f32_16x16x32_bf16 v[128:131], a[48:51], v[152:155], v[128:131]\n\t"
        "ds_read_b64_tr_b16 a[108:109], v11 offset:12384\n\t"
        "ds_read_b64_tr_b16 a[110:111], v11 offset:12896\n\t"
        "s_add_u32 s52, s52, 0x7000\n\t"
        "s_add_u32 s62, s62, 1\n\t"
        "s_cmp_lt_u32 s62, %[nld]\n\t"
        "s_cselect_b32 s38, s59, 0\n\t"
        "s_add_u32 s56, s60, 0\n\t"
        "s_waitcnt lgkmcnt(4)\n\t"
        "v_mfma_f32_16x16x32_bf16 v[128:131], a[52:55], v[156:159], v[128:131]\n\t"
        "ds_read_b64_tr_b16 a[160:161], v11 offset:16512\n\t"
        "ds_read_b64_tr_b16 a[162:163], v11 offset:17024\n\t"
        "s_mov_b32 m0, s56\n\t"
        "s_nop 0\n\t"
        "buffer_load_dwordx4 v1, s[36:39], s52 offen lds\n\t"
        "s_waitcnt lgkmcnt(4)\n\t"
        "v_mfma_f32_16x16x32_bf16 v[132:135], a[104:107], v[152:155], v[132:135]\n\t"
        "s_waitcnt lgkmcnt(2)\n\t"
        "ds_read_b64_tr_b16 a[164:165], v11 offset:20640\n\t"
        "ds_read_b64_tr_b16 a[166:167], v11 offset:21152\n\t"
        "v_mfma_f32_16x16x32_bf16 v[132:135], a[108:111], v[156:159], v[132:135]\n\t"
        "ds_read_b64_tr_b16 a[216:217], v11 offset:8\n\t"
        "ds_read_b64_tr_b16 a[218:219], v11 offset:520\n\t"
        "s_add_u32 m0, s56, 0x800\n\t"
        "s_nop 0\n\t"
        "buffer_load_dwordx4 v2, s[36:39], s52 offen lds\n\t"
        "s_waitcnt lgkmcnt(4)\n\t"
        "v_mfma_f32_16x16x32_bf16 v[136:139], a[160:163], v[152:155], v[136:139]\n\t"
        "ds_read_b64_tr_b16 a[220:221], v11 offset:4136\n\t"
        "ds_read_b64_tr_b16 a[222:223], v11 offset:4648\n\t"
        "s_add_u32 m0, s56, 0x1000\n\t"
        "s_nop 0\n\t"
        "buffer_load_dwordx4 v3, s[36:39], s52 offen lds\n\t"
        "s_waitcnt lgkmcnt(4)\n\t"
        "v_mfma_f32_16x16x32_bf16 v[136:139], a[164:167], v[156:159], v[136:139]\n\t"
        "s_waitcnt lgkmcnt(2)\n\t"
        "ds_read_b64_tr_b16 v[192:193], v11 offset:8264\n\t"
        "ds_read_b64_tr_b16 v[194:195], v11 offset:8776\n\t"
        "v_mfma_f32_16x16x32_bf16 v[140:143], a[216:219], v[152:155], v[140:143]\n\t"
        "ds_read_b64_tr_b16 v[196:197], v11 offset:12392\n\t"
        "ds_read_b64_tr_b16 v[198:199], v11 offset:12904\n\t"
        "s_add_u32 m0, s56, 0x1800\n\t"
        "s_nop 0\n\t"
        "buffer_load_dwordx4 v4, s[36:39], s52 offen lds\n\t"
        "s_waitcnt lgkmcnt(4)\n\t"
        "v_mfma_f32_16x16x32_bf16 v[140:143], a[220:223], v[156:159], v[140:143]\n\t"
        "ds_read_b64_tr_b16 v[248:249], v11 offset:16520\n\t"
        "ds_read_b64_tr_b16 v[250:251], v11 offset:17032\n\t"
        "s_add_u32 m0, s56, 0x2000\n\t"
        "s_nop 0\n\t"
        "buffer_load_dwordx4 v5, s[36:39], s52 offen lds\n\t"
        "s_waitcnt lgkmcnt(4)\n\t"
        "v_mfma_f32_16x16x32_bf16 v[144:147], v[192:195], v[152:155], v[144:147]\n\t"
        "s_waitcnt lgkmcnt(2)\n\t"
        "ds_read_b64_tr_b16 v[252:253], v11 offset:20648\n\t"
        "ds_read_b64_tr_b16 v[254:255], v11 offset:21160\n\t"
        "v_mfma_f32_16x16x32_bf16 v[144:147], v[196:199], v[156:159], v[144:147]\n\t"
        "s_add_u32 m0, s56, 0x2800\n\t"
        "s_nop 0\n\t"
        "buffer_load_dwordx4 v6, s[36:39], s52 offen lds\n\t"
        "s_waitcnt lgkmcnt(2)\n\t"
        "v_mfma_f32_16x16x32_bf16 v[148:151], v[248:251], v[152:155], v[148:151]\n\t"
        "s_add_u32 m0, s56, 0x3000\n\t"
        "s_nop 0\n\t"
        "buffer_load_dwordx4 v7, s[36:39], s52 offen lds\n\t"
        "s_waitcnt lgkmcnt(0)\n\t"
        "v_mfma_f32_16x16x32_bf16 v[148:151], v[252:255], v[156:159], v[148:151]\n\t"
        "v_add_u32 v8, 0x26000, v164\n\t"
        "v_add_u32 v9, 0x26000, v165\n\t"
        "s_mov_b32 s68, s36\n\t"
        "s_mov_b32 s69, s37\n\t"
        "s_mov_b32 s70, s59\n\t"
        "s_mov_b32 s71, s39\n\t"
        "v_mfma_f32_16x16x32_bf16 v[104:107], a[0:3], v[48:51], 0\n\t"
        "s_add_u32 s52, s52, 0x7000\n\t"
        "s_add_u32 s62, s62, 1\n\t"
        "s_cmp_lt_u32 s62, %[nld]\n\t"
        "v_mfma_f32_16x16x32_bf16 v[104:107], a[4:7], v[52:55], v[104:107]\n\t"
        "s_cselect_b32 s38, s59, 0\n\t"
        "s_add_u32 s56, s60, 0x7100\n\t"
        "v_mfma_f32_16x16x32_bf16 v[104:107], a[8:11], v[56:59], v[104:107]\n\t"
        "v_mfma_f32_16x16x32_bf16 v[104:107], a[12:15], v[60:63], v[104:107]\n\t"
        "s_mov_b32 m0, s56\n\t"
        "v_mfma_f32_16x16x32_bf16 v[104:107], a[16:19], v[64:67], v[104:107]\n\t"
        "buffer_load_dwordx4 v1, s[36:39], s52 offen lds\n\t"
        "v_mfma_f32_16x16x32_bf16 v[104:107], a[20:23], v[68:71], v[104:107]\n\t"
        "v_mfma_f32_16x16x32_bf16 v[104:107], a[24:27], v[72:75], v[104:107]\n\t"
        "v_fma_f32 v132, v132, v32, v40\n\t"
        "v_fma_f32 v133, v133, v33, v41\n\t"
        "v_mfma_f32_16x16x32_bf16 v[104:107], a[28:31], v[76:79], v[104:107]\n\t"
        "v_fma_f32 v134, v134, v34, v42\n\t"
        "v_fma_f32 v135, v135, v35, v43\n\t"
        "v_mfma_f32_16x16x32_bf16 v[104:107], a[32:35], v[80:83], v[104:107]\n\t"
        "s_add_u32 m0, s56, 0x800\n\t"
        "v_fma_f32 v144, v144, v36, v44\n\t"
        "v_fma_f32 v145, v145, v37, v45\n\t"
        "v_mfma_f32_16x16x32_bf16 v[104:107], a[36:39], v[84:87], v[104:107]\n\t"
        "buffer_load_dwordx4 v2, s[36:39], s52 offen lds\n\t"
        "v_fma_f32 v146, v146, v38, v46\n\t"
        "v_mfma_f32_16x16x32_bf16 v[104:107], a[40:43], v[88:91], v[104:107]\n\t"
        "v_fma_f32 v147, v147, v39, v47\n\t"
        "v_fmac_f32_dpp v132, v128, v32 row_shr:1 row_mask:0xf bank_mask:0xf bound_ctrl:1\n\t"
        "v_mfma_f32_16x16x32_bf16 v[104:107], a[44:47], v[92:95], v[104:107]\n\t"
        "v_fmac_f32_dpp v133, v129, v33 row_shr:1 row_mask:0xf bank_mask:0xf bound_ctrl:1\n\t"
        "v_fmac_f32_dpp v134, v130, v34 row_shr:1 row_mask:0xf bank_mask:0xf bound_ctrl:1\n\t"
        "v_mfma_f32_16x16x32_bf16 v[104:107], a[48:51], v[96:99], v[104:107]\n\t"
        "v_fmac_f32_dpp v135, v131, v35 row_shr:1 row_mask:0xf bank_mask:0xf bound_ctrl:1\n\t"
        "v_fmac_f32_dpp v144, v140, v36 row_shr:1 row_mask:0xf bank_mask:0xf bound_ctrl:1\n\t"
        "v_mfma_f32_16x16x32_bf16 v[104:107], a[52:55], v[100:103], v[104:107]\n\t"
        "s_add_u32 m0, s56, 0x1000\n\t"
        "v_fmac_f32_dpp v145, v141, v37 row_shr:1 row_mask:0xf bank_mask:0xf bound_ctrl:1\n\t"
        "v_fmac_f32_dpp v146, v142, v38 row_shr:1 row_mask:0xf bank_mask:0xf bound_ctrl:1\n\t"
        "v_mfma_f32_16x16x32_bf16 v[108:111], a[56:59], v[48:51], 0\n\t"
        "buffer_load_dwordx4 v3, s[36:39], s52 offen lds\n\t"
        "v_fmac_f32_dpp v147, v143, v39 row_shr:1 row_mask:0xf bank_mask:0xf bound_ctrl:1\n\t"
        "v_mfma_f32_16x16x32_bf16 v[108:111], a[60:63], v[52:55], v[108:111]\n\t"
        "v_fmac_f32_dpp v132, v136, v32 row_shl:1 row_mask:0xf bank_mask:0xf bound_ctrl:1\n\t"
        "v_fmac_f32_dpp v133, v137, v33 row_shl:1 row_mask:0xf bank_mask:0xf bound_ctrl:1\n\t"
        "v_mfma_f32_16x16x32_bf16 v[108:111], a[64:67], v[56:59], v[108:111]\n\t"
        "v_fmac_f32_dpp v134, v138, v34 row_shl:1 row_mask:0xf bank_mask:0xf bound_ctrl:1\n\t"
        "v_fmac_f32_dpp v135, v139, v35 row_shl:1 row_mask:0xf bank_mask:0xf bound_ctrl:1\n\t"
        "v_mfma_f32_16x16x32_bf16 v[108:111], a[68:71], v[60:63], v[108:111]\n\t"
        "v_fmac_f32_dpp v144, v148, v36 row_shl:1 row_mask:0xf bank_mask:0xf bound_ctrl:1\n\t"
        "v_fmac_f32_dpp v145, v149, v37 row_shl:1 row_mask:0xf bank_mask:0xf bound_ctrl:1\n\t"
        "v_mfma_f32_16x16x32_bf16 v[108:111], a[72:75], v[64:67], v[108:111]\n\t"
        "s_add_u32 m0, s56, 0x1800\n\t"
        "v_fmac_f32_dpp v146, v150, v38 row_shl:1 row_mask:0xf bank_mask:0xf bound_ctrl:1\n\t"
        "v_fmac_f32_dpp v147, v151, v39 row_shl:1 row_mask:0xf bank_mask:0xf bound_ctrl:1\n\t"
        "v_mfma_f32_16x16x32_bf16 v[108:111], a[76:79], v[68:71], v[108:111]\n\t"
        "buffer_load_dwordx4 v4, s[36:39], s52 offen lds\n\t"
        "v_exp_f32 v28, v132\n\t"
        "v_mfma_f32_16x16x32_bf16 v[108:111], a[80:83], v[72:75], v[108:111]\n\t"
        "v_exp_f32 v29, v133\n\t"
        "v_mfma_f32_16x16x32_bf16 v[108:111], a[84:87], v[76:79], v[108:111]\n\t"
        "v_exp_f32 v30, v134\n\t"
        "v_mfma_f32_16x16x32_bf16 v[108:111], a[88:91], v[80:83], v[108:111]\n\t"
        "v_exp_f32 v31, v135\n\t"
        "v_mfma_f32_16x16x32_bf16 v[108:111], a[92:95], v[84:87], v[108:111]\n\t"
        "s_add_u32 m0, s56, 0x2000\n\t"
        "v_exp_f32 v0, v144\n\t"
        "v_mfma_f32_16x16x32_bf16 v[108:111], a[96:99], v[88:91], v[108:111]\n\t"
        "buffer_load_dwordx4 v5, s[36:39], s52 offen lds\n\t"
        "v_exp_f32 v17, v145\n\t"
        "v_mfma_f32_16x16x32_bf16 v[108:111], a[100:103], v[92:95], v[108:111]\n\t"
        "v_exp_f32 v18, v146\n\t"
        "v_mfma_f32_16x16x32_bf16 v[108:111], a[104:107], v[96:99], v[108:111]\n\t"
        "v_exp_f32 v19, v147\n\t"
        "v_mfma_f32_16x16x32_bf16 v[108:111], a[108:111], v[100:103], v[108:111]\n\t"
        "v_fma_f32 v28, v28, s61, s61\n\t"
        "v_fma_f32 v29, v29, s61, s61\n\t"
        "v_mfma_f32_16x16x32_bf16 v[112:115], a[112:115], v[48:51], 0\n\t"
        "s_add_u32 m0, s56, 0x2800\n\t"
        "v_fma_f32 v30, v30, s61, s61\n\t"
        "v_fma_f32 v31, v31, s61, s61\n\t"
        "v_mfma_f32_16x16x32_bf16 v[112:115], a[116:119], v[52:55], v[112:115]\n\t"
        "buffer_load_dwordx4 v6, s[36:39], s52 offen lds\n\t"
        "v_fma_f32 v0, v0, s61, s61\n\t"
        "v_mfma_f32_16x16x32_bf16 v[112:115], a[120:123], v[56:59], v[112:115]\n\t"
        "v_fma_f32 v17, v17, s61, s61\n\t"
        "v_fma_f32 v18, v18, s61, s61\n\t"
        "v_mfma_f32_16x16x32_bf16 v[112:115], a[124:127], v[60:63], v[112:115]\n\t"
        "v_fma_f32 v19, v19, s61, s61\n\t"
        "v_mfma_f32_16x16x32_bf16 v[112:115], a[128:131], v[64:67], v[112:115]\n\t"
        "v_rcp_f32 v28, v28\n\t"
        "v_mfma_f32_16x16x32_bf16 v[112:115], a[132:135], v[68:71], v[112:115]\n\t"
        "s_add_u32 m0, s56, 0x3000\n\t"
        "s_add_u32 s50, s52, 0xe000\n\t"
        "v_rcp_f32 v29, v29\n\t"
        "v_mfma_f32_16x16x32_bf16 v[112:115], a[136:139], v[72:75], v[112:115]\n\t"
        "buffer_load_dwordx4 v7, s[36:39], s52 offen lds\n\t"
        "v_fma_f32 v108, v108, v32, v40\n\t"
        "v_mfma_f32_16x16x32_bf16 v[112:115], a[140:143], v[76:79], v[112:115]\n\t"
        "s_min_u32 s50, s50, 0x5b000\n\t"
        "v_rcp_f32 v30, v30\n\t"
        "v_mfma_f32_16x16x32_bf16 v[112:115], a[144:147], v[80:83], v[112:115]\n\t"
        "v_rcp_f32 v31, v31\n\t"
        "v_mfma_f32_16x16x32_bf16 v[112:115], a[148:151], v[84:87], v[112:115]\n\t"
        "buffer_load_dword v170, v1, s[68:71], s50 offen\n\t"
        "v_fma_f32 v109, v109, v33, v41\n\t"
        "v_mfma_f32_16x16x32_bf16 v[112:115], a[152:155], v[88:91], v[112:115]\n\t"
        "v_rcp_f32 v0, v0\n\t"
        "v_mfma_f32_16x16x32_bf16 v[112:115], a[156:159], v[92:95], v[112:115]\n\t"
        "v_rcp_f32 v17, v17\n\t"
        "v_mfma_f32_16x16x32_bf16 v[112:115], a[160:163], v[96:99], v[112:115]\n\t"
        "s_mov_b32 s48, 0x1c400\n\t"
        "v_rcp_f32 v18, v18\n\t"
        "v_mfma_f32_16x16x32_bf16 v[112:115], a[164:167], v[100:103], v[112:115]\n\t"
        "s_mov_b32 s49, 0x7100\n\t"
        "v_rcp_f32 v19, v19\n\t"
        "v_mfma_f32_16x16x32_bf16 v[116:119], a[168:171], v[48:51], 0\n\t"
        "v_mul_f32 v132, v132, v28\n\t"
        "v_mul_f32 v133, v133, v29\n\t"
        "v_mfma_f32_16x16x32_bf16 v[116:119], a[172:175], v[52:55], v[116:119]\n\t"
        "v_mul_f32 v134, v134, v30\n\t"
        "v_mul_f32 v135, v135, v31\n\t"
        "v_mfma_f32_16x16x32_bf16 v[116:119], a[176:179], v[56:59], v[116:119]\n\t"
        "v_mul_f32 v144, v144, v0\n\t"
        "v_mul_f32 v145, v145, v17\n\t"
        "v_mfma_f32_16x16x32_bf16 v[116:119], a[180:183], v[60:63], v[116:119]\n\t"
        "v_mul_f32 v146, v146, v18\n\t"
        "v_mul_f32 v147, v147, v19\n\t"
        "v_mfma_f32_16x16x32_bf16 v[116:119], a[184:187], v[64:67], v[116:119]\n\t"
        "v_cvt_pk_bf16_f32 v20, v132, v133\n\t"
        "v_cvt_pk_bf16_f32 v21, v134, v135\n\t"
        "v_mfma_f32_16x16x32_bf16 v[116:119], a[188:191], v[68:71], v[116:119]\n\t"
        "v_cvt_pk_bf16_f32 v22, v144, v145\n\t"
        "v_cvt_pk_bf16_f32 v23, v146, v147\n\t"
        "v_mfma_f32_16x16x32_bf16 v[116:119], a[192:195], v[72:75], v[116:119]\n\t"
        "ds_write_b128 v8, v[20:23]\n\t"
        "v_xor_b32 v8, 0x1000, v8\n\t"
        "v_mfma_f32_16x16x32_bf16 v[116:119], a[196:199], v[76:79], v[116:119]\n\t"
        "v_fma_f32 v110, v110, v34, v42\n\t"
        "v_fma_f32 v111, v111, v35, v43\n\t"
        "v_mfma_f32_16x16x32_bf16 v[116:119], a[200:203], v[80:83], v[116:119]\n\t"
        "v_fmac_f32_dpp v108, v104, v32 row_shr:1 row_mask:0xf bank_mask:0xf bound_ctrl:1\n\t"
        "v_fmac_f32_dpp v109, v105, v33 row_shr:1 row_mask:0xf bank_mask:0xf bound_ctrl:1\n\t"
        "v_mfma_f32_16x16x32_bf16 v[116:119], a[204:207], v[84:87], v[116:119]\n\t"
        "v_fmac_f32_dpp v110, v106, v34 row_shr:1 row_mask:0xf bank_mask:0xf bound_ctrl:1\n\t"
        "v_fmac_f32_dpp v111, v107, v35 row_shr:1 row_mask:0xf bank_mask:0xf bound_ctrl:1\n\t"
        "v_mfma_f32_16x16x32_bf16 v[116:119], a[208:211], v[88:91], v[116:119]\n\t"
        "v_fmac_f32_dpp v108, v112, v32 row_shl:1 row_mask:0xf bank_mask:0xf bound_ctrl:1\n\t"
        "v_fmac_f32_dpp v109, v113, v33 row_shl:1 row_mask:0xf bank_mask:0xf bound_ctrl:1\n\t"
        "v_mfma_f32_16x16x32_bf16 v[116:119], a[212:215], v[92:95], v[116:119]\n\t"
        "v_fmac_f32_dpp v110, v114, v34 row_shl:1 row_mask:0xf bank_mask:0xf bound_ctrl:1\n\t"
        "v_fmac_f32_dpp v111, v115, v35 row_shl:1 row_mask:0xf bank_mask:0xf bound_ctrl:1\n\t"
        "v_mfma_f32_16x16x32_bf16 v[116:119], a[216:219], v[96:99], v[116:119]\n\t"
        "v_exp_f32 v28, v108\n\t"
        "v_mfma_f32_16x16x32_bf16 v[116:119], a[220:223], v[100:103], v[116:119]\n\t"
        "v_exp_f32 v29, v109\n\t"
        "v_mfma_f32_16x16x32_bf16 v[120:123], a[224:227], v[48:51], 0\n\t"
        "v_exp_f32 v30, v110\n\t"
        "v_mfma_f32_16x16x32_bf16 v[120:123], a[228:231], v[52:55], v[120:123]\n\t"
        "s_add_u32 s63, s48, s58\n\t"
        "v_exp_f32 v31, v111\n\t"
        "v_mfma_f32_16x16x32_bf16 v[120:123], a[232:235], v[56:59], v[120:123]\n\t"
        "v_fma_f32 v28, v28, s61, s61\n\t"
        "v_fma_f32 v29, v29, s61, s61\n\t"
        "v_mfma_f32_16x16x32_bf16 v[120:123], a[236:239], v[60:63], v[120:123]\n\t"
        "v_fma_f32 v30, v30, s61, s61\n\t"
        "v_fma_f32 v31, v31, s61, s61\n\t"
        "v_mfma_f32_16x16x32_bf16 v[120:123], a[240:243], v[64:67], v[120:123]\n\t"
        "v_rcp_f32 v28, v28\n\t"
        "v_mfma_f32_16x16x32_bf16 v[120:123], a[244:247], v[68:71], v[120:123]\n\t"
        "v_add_u32 v14, s63, v12\n\t"
        "v_mfma_f32_16x16x32_bf16 v[120:123], a[248:251], v[72:75], v[120:123]\n\t"
        "v_xor_b32 v15, 64, v14\n\t"
        "v_mfma_f32_16x16x32_bf16 v[120:123], a[252:255], v[76:79], v[120:123]\n\t"
        "v_rcp_f32 v29, v29\n\t"
        "v_mfma_f32_16x16x32_bf16 v[120:123], v[176:179], v[80:83], v[120:123]\n\t"
        "v_rcp_f32 v30, v30\n\t"
        "v_mfma_f32_16x16x32_bf16 v[120:123], v[180:183], v[84:87], v[120:123]\n\t"
        "v_rcp_f32 v31, v31\n\t"
        "v_mfma_f32_16x16x32_bf16 v[120:123], v[184:187], v[88:91], v[120:123]\n\t"
        "v_mul_f32 v108, v108, v28\n\t"
        "v_mul_f32 v109, v109, v29\n\t"
        "v_mfma_f32_16x16x32_bf16 v[120:123], v[188:191], v[92:95], v[120:123]\n\t"
        "s_waitcnt lgkmcnt(0)\n\t"
        "v_mul_f32 v110, v110, v30\n\t"
        "v_mul_f32 v111, v111, v31\n\t"
        "v_mfma_f32_16x16x32_bf16 v[120:123], v[192:195], v[96:99], v[120:123]\n\t"
        "s_waitcnt vmcnt(15)\n\t"
        "s_barrier\n\t"
        "v_mfma_f32_16x16x32_bf16 v[120:123], v[196:199], v[100:103], v[120:123]\n\t"
        "ds_read_b128 v[160:163], v9\n\t"
        "v_cvt_pk_bf16_f32 v20, v108, v109\n\t"
        "v_mfma_f32_16x16x32_bf16 v[124:127], v[200:203], v[48:51], 0\n\t"
        "ds_read_b128 v[48:51], v14\n\t"
        "v_cvt_pk_bf16_f32 v21, v110, v111\n\t"
        "v_mfma_f32_16x16x32_bf16 v[124:127], v[204:207], v[52:55], v[124:127]\n\t"
        "ds_read_b128 v[52:55], v15\n\t"
        "v_mfma_f32_16x16x32_bf16 v[124:127], v[208:211], v[56:59], v[124:127]\n\t"
        "ds_read_b128 v[56:59], v14 offset:2048\n\t"
        "v_xor_b32 v9, 0x1000, v9\n\t"
        "v_mfma_f32_16x16x32_bf16 v[124:127], v[212:215], v[60:63], v[124:127]\n\t"
        "ds_read_b128 v[60:63], v15 offset:2048\n\t"
        "v_mfma_f32_16x16x32_bf16 v[124:127], v[216:219], v[64:67], v[124:127]\n\t"
        "ds_read_b128 v[64:67], v14 offset:4096\n\t"
        "v_mfma_f32_16x16x32_bf16 v[124:127], v[220:223], v[68:71], v[124:127]\n\t"
        "ds_read_b128 v[68:71], v15 offset:4096\n\t"
        "v_mfma_f32_16x16x32_bf16 v[124:127], v[224:227], v[72:75], v[124:127]\n\t"
        "ds_read_b128 v[72:75], v14 offset:6144\n\t"
        "v_mfma_f32_16x16x32_bf16 v[124:127], v[228:231], v[76:79], v[124:127]\n\t"
        "s_waitcnt lgkmcnt(6)\n\t"
        "ds_read_b128 v[76:79], v15 offset:6144\n\t"
        "buffer_store_dwordx4 v[160:163], v13, %[osrd], s53 offen sc1\n\t"
        "v_mfma_f32_16x16x32_bf16 v[124:127], v[232:235], v[80:83], v[124:127]\n\t"
        "s_add_u32 s53, s53, 0x7000\n\t"
        "ds_read_b128 v[80:83], v14 offset:8192\n\t"
        "v_mfma_f32_16x16x32_bf16 v[124:127], v[236:239], v[84:87], v[124:127]\n\t"
        "ds_read_b128 v[84:87], v15 offset:8192\n\t"
        "v_mfma_f32_16x16x32_bf16 v[124:127], v[240:243], v[88:91], v[124:127]\n\t"
        "ds_read_b128 v[88:91], v14 offset:10240\n\t"
        "v_mfma_f32_16x16x32_bf16 v[124:127], v[244:247], v[92:95], v[124:127]\n\t"
        "ds_read_b128 v[92:95], v15 offset:10240\n\t"
        "v_mfma_f32_16x16x32_bf16 v[124:127], v[248:251], v[96:99], v[124:127]\n\t"
        "ds_read_b128 v[96:99], v14 offset:12288\n\t"
        "v_mfma_f32_16x16x32_bf16 v[124:127], v[252:255], v[100:103], v[124:127]\n\t"
        "s_waitcnt lgkmcnt(6)\n\t"
        "ds_read_b128 v[100:103], v15 offset:12288\n\t"
        "s_mov_b32 s57, 12\n\t"
        "LOOP0%=:\n\t"
        "v_mfma_f32_16x16x32_bf16 v[104:107], a[0:3], v[48:51], 0\n\t"
        "s_add_u32 s52, s52, 0x7000\n\t"
        "s_add_u32 s62, s62, 1\n\t"
        "s_cmp_lt_u32 s62, %[nld]\n\t"
        "v_mfma_f32_16x16x32_bf16 v[104:107], a[4:7], v[52:55], v[104:107]\n\t"
        "s_cselect_b32 s38, s59, 0\n\t"
        "s_add_u32 s49, s49, 0x7100\n\t"
        "s_cmp_eq_u32 s49, 0x23500\n\t"
        "v_mfma_f32_16x16x32_bf16 v[104:107], a[8:11], v[56:59], v[104:107]\n\t"
        "s_cselect_b32 s49, 0, s49\n\t"
        "s_add_u32 s56, s49, s60\n\t"
        "v_mfma_f32_16x16x32_bf16 v[104:107], a[12:15], v[60:63], v[104:107]\n\t"
        "s_mov_b32 m0, s56\n\t"
        "v_mfma_f32_16x16x32_bf16 v[104:107], a[16:19], v[64:67], v[104:107]\n\t"
        "buffer_load_dwordx4 v1, s[36:39], s52 offen lds\n\t"
        "v_fma_f32 v120, v120, v36, v44\n\t"
        "v_mfma_f32_16x16x32_bf16 v[104:107], a[20:23], v[68:71], v[104:107]\n\t"
        "v_fma_f32 v121, v121, v37, v45\n\t"
        "v_fma_f32 v122, v122, v38, v46\n\t"
        "v_mfma_f32_16x16x32_bf16 v[104:107], a[24:27], v[72:75], v[104:107]\n\t"
        "s_waitcnt lgkmcnt(3)\n\t"
        "v_fma_f32 v123, v123, v39, v47\n\t"
        "v_fmac_f32_dpp v120, v116, v36 row_shr:1 row_mask:0xf bank_mask:0xf bound_ctrl:1\n\t"
        "v_mfma_f32_16x16x32_bf16 v[104:107], a[28:31], v[76:79], v[104:107]\n\t"
        "v_fmac_f32_dpp v121, v117, v37 row_shr:1 row_mask:0xf bank_mask:0xf bound_ctrl:1\n\t"
        "v_fmac_f32_dpp v122, v118, v38 row_shr:1 row_mask:0xf bank_mask:0xf bound_ctrl:1\n\t"
        "v_mfma_f32_16x16x32_bf16 v[104:107], a[32:35], v[80:83], v[104:107]\n\t"
        "s_add_u32 m0, s56, 0x800\n\t"
        "v_fmac_f32_dpp v123, v119, v39 row_shr:1 row_mask:0xf bank_mask:0xf bound_ctrl:1\n\t"
        "v_fmac_f32_dpp v120, v124, v36 row_shl:1 row_mask:0xf bank_mask:0xf bound_ctrl:1\n\t"
        "v_mfma_f32_16x16x32_bf16 v[104:107], a[36:39], v[84:87], v[104:107]\n\t"
        "buffer_load_dwordx4 v2, s[36:39], s52 offen lds\n\t"
        "v_fmac_f32_dpp v121, v125, v37 row_shl:1 row_mask:0xf bank_mask:0xf bound_ctrl:1\n\t"
        "v_mfma_f32_16x16x32_bf16 v[104:107], a[40:43], v[88:91], v[104:107]\n\t"
        "s_waitcnt lgkmcnt(0)\n\t"
        "v_fmac_f32_dpp v122, v126, v38 row_shl:1 row_mask:0xf bank_mask:0xf bound_ctrl:1\n\t"
        "v_fmac_f32_dpp v123, v127, v39 row_shl:1 row_mask:0xf bank_mask:0xf bound_ctrl:1\n\t"
        "v_mfma_f32_16x16x32_bf16 v[104:107], a[44:47], v[92:95], v[104:107]\n\t"
        "v_exp_f32 v28, v120\n\t"
        "v_mfma_f32_16x16x32_bf16 v[104:107], a[48:51], v[96:99], v[104:107]\n\t"
        "v_exp_f32 v29, v121\n\t"
        "v_mfma_f32_16x16x32_bf16 v[104:107], a[52:55], v[100:103], v[104:107]\n\t"
        "s_add_u32 m0, s56, 0x1000\n\t"
        "v_exp_f32 v30, v122\n\t"
        "v_mfma_f32_16x16x32_bf16 v[108:111], a[56:59], v[48:51], 0\n\t"
        "buffer_load_dwordx4 v3, s[36:39], s52 offen lds\n\t"
        "v_mfma_f32_16x16x32_bf16 v[108:111], a[60:63], v[52:55], v[108:111]\n\t"
        "v_exp_f32 v31, v123\n\t"
        "v_mfma_f32_16x16x32_bf16 v[108:111], a[64:67], v[56:59], v[108:111]\n\t"
        "v_fma_f32 v28, v28, s61, s61\n\t"
        "v_fma_f32 v29, v29, s61, s61\n\t"
        "v_mfma_f32_16x16x32_bf16 v[108:111], a[68:71], v[60:63], v[108:111]\n\t"
        "v_fma_f32 v30, v30, s61, s61\n\t"
        "v_fma_f32 v31, v31, s61, s61\n\t"
        "v_mfma_f32_16x16x32_bf16 v[108:111], a[72:75], v[64:67], v[108:111]\n\t"
        "s_add_u32 m0, s56, 0x1800\n\t"
        "v_rcp_f32 v28, v28\n\t"
        "v_mfma_f32_16x16x32_bf16 v[108:111], a[76:79], v[68:71], v[108:111]\n\t"
        "buffer_load_dwordx4 v4, s[36:39], s52 offen lds\n\t"
        "v_mfma_f32_16x16x32_bf16 v[108:111], a[80:83], v[72:75], v[108:111]\n\t"
        "v_rcp_f32 v29, v29\n\t"
        "v_mfma_f32_16x16x32_bf16 v[108:111], a[84:87], v[76:79], v[108:111]\n\t"
        "v_rcp_f32 v30, v30\n\t"
        "v_mfma_f32_16x16x32_bf16 v[108:111], a[88:91], v[80:83], v[108:111]\n\t"
        "v_rcp_f32 v31, v31\n\t"
        "v_mfma_f32_16x16x32_bf16 v[108:111], a[92:95], v[84:87], v[108:111]\n\t"
        "s_add_u32 m0, s56, 0x2000\n\t"
        "v_mul_f32 v120, v120, v28\n\t"
        "v_mul_f32 v121, v121, v29\n\t"
        "v_mfma_f32_16x16x32_bf16 v[108:111], a[96:99], v[88:91], v[108:111]\n\t"
        "buffer_load_dwordx4 v5, s[36:39], s52 offen lds\n\t"
        "v_mul_f32 v122, v122, v30\n\t"
        "v_mfma_f32_16x16x32_bf16 v[108:111], a[100:103], v[92:95], v[108:111]\n\t"
        "v_mul_f32 v123, v123, v31\n\t"
        "v_cvt_pk_bf16_f32 v22, v120, v121\n\t"
        "v_mfma_f32_16x16x32_bf16 v[108:111], a[104:107], v[96:99], v[108:111]\n\t"
        "v_cvt_pk_bf16_f32 v23, v122, v123\n\t"
        "v_mfma_f32_16x16x32_bf16 v[108:111], a[108:111], v[100:103], v[108:111]\n\t"
        "ds_write_b128 v8, v[20:23]\n\t"
        "v_xor_b32 v8, 0x1000, v8\n\t"
        "v_mfma_f32_16x16x32_bf16 v[112:115], a[112:115], v[48:51], 0\n\t"
        "s_add_u32 m0, s56, 0x2800\n\t"
        "v_mfma_f32_16x16x32_bf16 v[112:115], a[116:119], v[52:55], v[112:115]\n\t"
        "buffer_load_dwordx4 v6, s[36:39], s52 offen lds\n\t"
        "v_mfma_f32_16x16x32_bf16 v[112:115], a[120:123], v[56:59], v[112:115]\n\t"
        "v_mfma_f32_16x16x32_bf16 v[112:115], a[124:127], v[60:63], v[112:115]\n\t"
        "v_mfma_f32_16x16x32_bf16 v[112:115], a[128:131], v[64:67], v[112:115]\n\t"
        "v_mfma_f32_16x16x32_bf16 v[112:115], a[132:135], v[68:71], v[112:115]\n\t"
        "s_add_u32 m0, s56, 0x3000\n\t"
        "s_add_u32 s50, s52, 0xe000\n\t"
        "v_mfma_f32_16x16x32_bf16 v[112:115], a[136:139], v[72:75], v[112:115]\n\t"
        "buffer_load_dwordx4 v7, s[36:39], s52 offen lds\n\t"
        "v_mfma_f32_16x16x32_bf16 v[112:115], a[140:143], v[76:79], v[112:115]\n\t"
        "s_min_u32 s50, s50, 0x5b000\n\t"
        "v_mfma_f32_16x16x32_bf16 v[112:115], a[144:147], v[80:83], v[112:115]\n\t"
        "v_mfma_f32_16x16x32_bf16 v[112:115], a[148:151], v[84:87], v[112:115]\n\t"
        "buffer_load_dword v170, v1, s[68:71], s50 offen\n\t"
        "v_mfma_f32_16x16x32_bf16 v[112:115], a[152:155], v[88:91], v[112:115]\n\t"
        "v_mfma_f32_16x16x32_bf16 v[112:115], a[156:159], v[92:95], v[112:115]\n\t"
        "v_mfma_f32_16x16x32_bf16 v[112:115], a[160:163], v[96:99], v[112:115]\n\t"
        "v_mfma_f32_16x16x32_bf16 v[112:115], a[164:167], v[100:103], v[112:115]\n\t"
        "v_mfma_f32_16x16x32_bf16 v[116:119], a[168:171], v[48:51], 0\n\t"
        "v_mfma_f32_16x16x32_bf16 v[116:119], a[172:175], v[52:55], v[116:119]\n\t"
        "v_mfma_f32_16x16x32_bf16 v[116:119], a[176:179], v[56:59], v[116:119]\n\t"
        "v_mfma_f32_16x16x32_bf16 v[116:119], a[180:183], v[60:63], v[116:119]\n\t"
        "v_mfma_f32_16x16x32_bf16 v[116:119], a[184:187], v[64:67], v[116:119]\n\t"
        "v_fma_f32 v108, v108, v32, v40\n\t"
        "v_fma_f32 v109, v109, v33, v41\n\t"
        "v_mfma_f32_16x16x32_bf16 v[116:119], a[188:191], v[68:71], v[116:119]\n\t"
        "v_fma_f32 v110, v110, v34, v42\n\t"
        "v_fma_f32 v111, v111, v35, v43\n\t"
        "v_mfma_f32_16x16x32_bf16 v[116:119], a[192:195], v[72:75], v[116:119]\n\t"
        "v_fmac_f32_dpp v108, v104, v32 row_shr:1 row_mask:0xf bank_mask:0xf bound_ctrl:1\n\t"
        "v_fmac_f32_dpp v109, v105, v33 row_shr:1 row_mask:0xf bank_mask:0xf bound_ctrl:1\n\t"
        "v_mfma_f32_16x16x32_bf16 v[116:119], a[196:199], v[76:79], v[116:119]\n\t"
        "v_fmac_f32_dpp v110, v106, v34 row_shr:1 row_mask:0xf bank_mask:0xf bound_ctrl:1\n\t"
        "v_fmac_f32_dpp v111, v107, v35 row_shr:1 row_mask:0xf bank_mask:0xf bound_ctrl:1\n\t"
        "v_mfma_f32_16x16x32_bf16 v[116:119], a[200:203], v[80:83], v[116:119]\n\t"
        "v_fmac_f32_dpp v108, v112, v32 row_shl:1 row_mask:0xf bank_mask:0xf bound_ctrl:1\n\t"
        "v_fmac_f32_dpp v109, v113, v33 row_shl:1 row_mask:0xf bank_mask:0xf bound_ctrl:1\n\t"
        "v_mfma_f32_16x16x32_bf16 v[116:119], a[204:207], v[84:87], v[116:119]\n\t"
        "v_fmac_f32_dpp v110, v114, v34 row_shl:1 row_mask:0xf bank_mask:0xf bound_ctrl:1\n\t"
        "v_fmac_f32_dpp v111, v115, v35 row_shl:1 row_mask:0xf bank_mask:0xf bound_ctrl:1\n\t"
        "v_mfma_f32_16x16x32_bf16 v[116:119], a[208:211], v[88:91], v[116:119]\n\t"
        "s_add_u32 s48, s48, 0x7100\n\t"
        "v_exp_f32 v28, v108\n\t"
        "v_mfma_f32_16x16x32_bf16 v[116:119], a[212:215], v[92:95], v[116:119]\n\t"
        "s_cmp_eq_u32 s48, 0x23500\n\t"
        "v_exp_f32 v29, v109\n\t"
        "v_mfma_f32_16x16x32_bf16 v[116:119], a[216:219], v[96:99], v[116:119]\n\t"
        "s_cselect_b32 s48, 0, s48\n\t"
        "v_exp_f32 v30, v110\n\t"
        "v_mfma_f32_16x16x32_bf16 v[116:119], a[220:223], v[100:103], v[116:119]\n\t"
        "s_cmp_eq_u32 s57, 1\n\t"
        "v_exp_f32 v31, v111\n\t"
        "v_mfma_f32_16x16x32_bf16 v[120:123], a[224:227], v[48:51], 0\n\t"
        "s_cselect_b32 s66, 0, s58\n\t"
        "v_fma_f32 v28, v28, s61, s61\n\t"
        "v_fma_f32 v29, v29, s61, s61\n\t"
        "v_mfma_f32_16x16x32_bf16 v[120:123], a[228:231], v[52:55], v[120:123]\n\t"
        "s_add_u32 s63, s48, s66\n\t"
        "v_fma_f32 v30, v30, s61, s61\n\t"
        "v_fma_f32 v31, v31, s61, s61\n\t"
        "v_mfma_f32_16x16x32_bf16 v[120:123], a[232:235], v[56:59], v[120:123]\n\t"
        "v_rcp_f32 v28, v28\n\t"
        "v_mfma_f32_16x16x32_bf16 v[120:123], a[236:239], v[60:63], v[120:123]\n\t"
        "v_rcp_f32 v29, v29\n\t"
        "v_mfma_f32_16x16x32_bf16 v[120:123], a[240:243], v[64:67], v[120:123]\n\t"
        "v_rcp_f32 v30, v30\n\t"
        "v_mfma_f32_16x16x32_bf16 v[120:123], a[244:247], v[68:71], v[120:123]\n\t"
        "v_add_u32 v14, s63, v12\n\t"
        "v_mfma_f32_16x16x32_bf16 v[120:123], a[248:251], v[72:75], v[120:123]\n\t"
        "v_xor_b32 v15, 64, v14\n\t"
        "v_mfma_f32_16x16x32_bf16 v[120:123], a[252:255], v[76:79], v[120:123]\n\t"
        "v_rcp_f32 v31, v31\n\t"
        "v_mfma_f32_16x16x32_bf16 v[120:123], v[176:179], v[80:83], v[120:123]\n\t"
        "v_mul_f32 v108, v108, v28\n\t"
        "v_mul_f32 v109, v109, v29\n\t"
        "v_mfma_f32_16x16x32_bf16 v[120:123], v[180:183], v[84:87], v[120:123]\n\t"
        "v_mul_f32 v110, v110, v30\n\t"
        "v_mul_f32 v111, v111, v31\n\t"
        "v_mfma_f32_16x16x32_bf16 v[120:123], v[184:187], v[88:91], v[120:123]\n\t"
        "v_cvt_pk_bf16_f32 v20, v108, v109\n\t"
        "v_cvt_pk_bf16_f32 v21, v110, v111\n\t"
        "v_mfma_f32_16x16x32_bf16 v[120:123], v[188:191], v[92:95], v[120:123]\n\t"
        "s_waitcnt lgkmcnt(0)\n\t"
        "v_mfma_f32_16x16x32_bf16 v[120:123], v[192:195], v[96:99], v[120:123]\n\t"
        "s_waitcnt vmcnt(17)\n\t"
        "s_barrier\n\t"
        "v_mfma_f32_16x16x32_bf16 v[120:123], v[196:199], v[100:103], v[120:123]\n\t"
        "ds_read_b128 v[160:163], v9\n\t"
        "v_mfma_f32_16x16x32_bf16 v[124:127], v[200:203], v[48:51], 0\n\t"
        "ds_read_b128 v[48:51], v14\n\t"
        "v_mfma_f32_16x16x32_bf16 v[124:127], v[204:207], v[52:55], v[124:127]\n\t"
        "ds_read_b128 v[52:55], v15\n\t"
        "v_mfma_f32_16x16x32_bf16 v[124:127], v[208:211], v[56:59], v[124:127]\n\t"
        "ds_read_b128 v[56:59], v14 offset:2048\n\t"
        "v_xor_b32 v9, 0x1000, v9\n\t"
        "v_mfma_f32_16x16x32_bf16 v[124:127], v[212:215], v[60:63], v[124:127]\n\t"
        "ds_read_b128 v[60:63], v15 offset:2048\n\t"
        "v_mfma_f32_16x16x32_bf16 v[124:127], v[216:219], v[64:67], v[124:127]\n\t"
        "ds_read_b128 v[64:67], v14 offset:4096\n\t"
        "v_mfma_f32_16x16x32_bf16 v[124:127], v[220:223], v[68:71], v[124:127]\n\t"
        "ds_read_b128 v[68:71], v15 offset:4096\n\t"
        "v_mfma_f32_16x16x32_bf16 v[124:127], v[224:227], v[72:75], v[124:127]\n\t"
        "ds_read_b128 v[72:75], v14 offset:6144\n\t"
        "v_mfma_f32_16x16x32_bf16 v[124:127], v[228:231], v[76:79], v[124:127]\n\t"
        "s_waitcnt lgkmcnt(6)\n\t"
        "ds_read_b128 v[76:79], v15 offset:6144\n\t"
        "buffer_store_dwordx4 v[160:163], v13, %[osrd], s53 offen sc1\n\t"
        "v_mfma_f32_16x16x32_bf16 v[124:127], v[232:235], v[80:83], v[124:127]\n\t"
        "s_add_u32 s53, s53, 0x7000\n\t"
        "ds_read_b128 v[80:83], v14 offset:8192\n\t"
        "v_mfma_f32_16x16x32_bf16 v[124:127], v[236:239], v[84:87], v[124:127]\n\t"
        "ds_read_b128 v[84:87], v15 offset:8192\n\t"
        "v_mfma_f32_16x16x32_bf16 v[124:127], v[240:243], v[88:91], v[124:127]\n\t"
        "ds_read_b128 v[88:91], v14 offset:10240\n\t"
        "v_mfma_f32_16x16x32_bf16 v[124:127], v[244:247], v[92:95], v[124:127]\n\t"
        "s_sub_u32 s57, s57, 1\n\t"
        "ds_read_b128 v[92:95], v15 offset:10240\n\t"
        "v_mfma_f32_16x16x32_bf16 v[124:127], v[248:251], v[96:99], v[124:127]\n\t"
        "s_cmp_lg_u32 s57, 0\n\t"
        "ds_read_b128 v[96:99], v14 offset:12288\n\t"
        "v_mfma_f32_16x16x32_bf16 v[124:127], v[252:255], v[100:103], v[124:127]\n\t"
        "s_waitcnt lgkmcnt(6)\n\t"
        "ds_read_b128 v[100:103], v15 offset:12288\n\t"
        "s_cbranch_scc1 LOOP0%=\n\t"
        "TAIL%=:\n\t"
        "s_cmp_eq_u32 %[extra], 0\n\t"
        "s_cbranch_scc1 TAIL0%=\n\t"
        "s_cmp_eq_u32 s64, 0\n\t"
        "s_cbranch_scc1 HALF0%=\n\t"
        "v_mfma_f32_16x16x32_bf16 v[104:107], a[168:171], v[48:51], 0\n\t"
        "v_mfma_f32_16x16x32_bf16 v[104:107], a[172:175], v[52:55], v[104:107]\n\t"
        "v_mfma_f32_16x16x32_bf16 v[104:107], a[176:179], v[56:59], v[104:107]\n\t"
        "v_mfma_f32_16x16x32_bf16 v[104:107], a[180:183], v[60:63], v[104:107]\n\t"
        "v_mfma_f32_16x16x32_bf16 v[104:107], a[184:187], v[64:67], v[104:107]\n\t"
        "v_fma_f32 v120, v120, v36, v44\n\t"
        "v_mfma_f32_16x16x32_bf16 v[104:107], a[188:191], v[68:71], v[104:107]\n\t"
        "v_fma_f32 v121, v121, v37, v45\n\t"
        "v_mfma_f32_16x16x32_bf16 v[104:107], a[192:195], v[72:75], v[104:107]\n\t"
        "s_waitcnt lgkmcnt(3)\n\t"
        "v_fma_f32 v122, v122, v38, v46\n\t"
        "v_mfma_f32_16x16x32_bf16 v[104:107], a[196:199], v[76:79], v[104:107]\n\t"
        "v_fma_f32 v123, v123, v39, v47\n\t"
        "v_mfma_f32_16x16x32_bf16 v[104:107], a[200:203], v[80:83], v[104:107]\n\t"
        "v_fmac_f32_dpp v120, v116, v36 row_shr:1 row_mask:0xf bank_mask:0xf bound_ctrl:1\n\t"
        "v_mfma_f32_16x16x32_bf16 v[104:107], a[204:207], v[84:87], v[104:107]\n\t"
        "v_fmac_f32_dpp v121, v117, v37 row_shr:1 row_mask:0xf bank_mask:0xf bound_ctrl:1\n\t"
        "v_mfma_f32_16x16x32_bf16 v[104:107], a[208:211], v[88:91], v[104:107]\n\t"
        "s_waitcnt lgkmcnt(0)\n\t"
        "v_fmac_f32_dpp v122, v118, v38 row_shr:1 row_mask:0xf bank_mask:0xf bound_ctrl:1\n\t"
        "v_mfma_f32_16x16x32_bf16 v[104:107], a[212:215], v[92:95], v[104:107]\n\t"
        "v_fmac_f32_dpp v123, v119, v39 row_shr:1 row_mask:0xf bank_mask:0xf bound_ctrl:1\n\t"
        "v_mfma_f32_16x16x32_bf16 v[104:107], a[216:219], v[96:99], v[104:107]\n\t"
        "v_fmac_f32_dpp v120, v124, v36 row_shl:1 row_mask:0xf bank_mask:0xf bound_ctrl:1\n\t"
        "v_mfma_f32_16x16x32_bf16 v[104:107], a[220:223], v[100:103], v[104:107]\n\t"
        "v_fmac_f32_dpp v121, v125, v37 row_shl:1 row_mask:0xf bank_mask:0xf bound_ctrl:1\n\t"
        "v_mfma_f32_16x16x32_bf16 v[108:111], a[224:227], v[48:51], 0\n\t"
        "v_fmac_f32_dpp v122, v126, v38 row_shl:1 row_mask:0xf bank_mask:0xf bound_ctrl:1\n\t"
        "v_mfma_f32_16x16x32_bf16 v[108:111], a[228:231], v[52:55], v[108:111]\n\t"
        "v_fmac_f32_dpp v123, v127, v39 row_shl:1 row_mask:0xf bank_mask:0xf bound_ctrl:1\n\t"
        "v_mfma_f32_16x16x32_bf16 v[108:111], a[232:235], v[56:59], v[108:111]\n\t"
        "v_exp_f32 v28, v120\n\t"
        "v_mfma_f32_16x16x32_bf16 v[108:111], a[236:239], v[60:63], v[108:111]\n\t"
        "v_exp_f32 v29, v121\n\t"
        "v_mfma_f32_16x16x32_bf16 v[108:111], a[240:243], v[64:67], v[108:111]\n\t"
        "v_exp_f32 v30, v122\n\t"
        "v_mfma_f32_16x16x32_bf16 v[108:111], a[244:247], v[68:71], v[108:111]\n\t"
        "v_exp_f32 v31, v123\n\t"
        "v_mfma_f32_16x16x32_bf16 v[108:111], a[248:251], v[72:75], v[108:111]\n\t"
        "v_fma_f32 v28, v28, s61, s61\n\t"
        "v_mfma_f32_16x16x32_bf16 v[108:111], a[252:255], v[76:79], v[108:111]\n\t"
        "v_fma_f32 v29, v29, s61, s61\n\t"
        "v_mfma_f32_16x16x32_bf16 v[108:111], v[176:179], v[80:83], v[108:111]\n\t"
        "v_fma_f32 v30, v30, s61, s61\n\t"
        "v_mfma_f32_16x16x32_bf16 v[108:111], v[180:183], v[84:87], v[108:111]\n\t"
        "v_fma_f32 v31, v31, s61, s61\n\t"
        "v_mfma_f32_16x16x32_bf16 v[108:111], v[184:187], v[88:91], v[108:111]\n\t"
        "v_rcp_f32 v28, v28\n\t"
        "v_mfma_f32_16x16x32_bf16 v[108:111], v[188:191], v[92:95], v[108:111]\n\t"
        "v_rcp_f32 v29, v29\n\t"
        "v_mfma_f32_16x16x32_bf16 v[108:111], v[192:195], v[96:99], v[108:111]\n\t"
        "v_rcp_f32 v30, v30\n\t"
        "v_mfma_f32_16x16x32_bf16 v[108:111], v[196:199], v[100:103], v[108:111]\n\t"
        "v_rcp_f32 v31, v31\n\t"
        "v_mfma_f32_16x16x32_bf16 v[112:115], v[200:203], v[48:51], 0\n\t"
        "v_mul_f32 v120, v120, v28\n\t"
        "v_mfma_f32_16x16x32_bf16 v[112:115], v[204:207], v[52:55], v[112:115]\n\t"
        "v_mul_f32 v121, v121, v29\n\t"
        "v_mfma_f32_16x16x32_bf16 v[112:115], v[208:211], v[56:59], v[112:115]\n\t"
        "v_mul_f32 v122, v122, v30\n\t"
        "v_mfma_f32_16x16x32_bf16 v[112:115], v[212:215], v[60:63], v[112:115]\n\t"
        "v_mul_f32 v123, v123, v31\n\t"
        "v_mfma_f32_16x16x32_bf16 v[112:115], v[216:219], v[64:67], v[112:115]\n\t"
        "v_cvt_pk_bf16_f32 v22, v120, v121\n\t"
        "v_mfma_f32_16x16x32_bf16 v[112:115], v[220:223], v[68:71], v[112:115]\n\t"
        "v_cvt_pk_bf16_f32 v23, v122, v123\n\t"
        "v_mfma_f32_16x16x32_bf16 v[112:115], v[224:227], v[72:75], v[112:115]\n\t"
        "buffer_store_dwordx4 v[20:23], v16, %[osrd], s53 offen\n\t"
        "v_mfma_f32_16x16x32_bf16 v[112:115], v[228:231], v[76:79], v[112:115]\n\t"
        "v_fma_f32 v108, v108, v36, v44\n\t"
        "v_mfma_f32_16x16x32_bf16 v[112:115], v[232:235], v[80:83], v[112:115]\n\t"
        "v_fma_f32 v109, v109, v37, v45\n\t"
        "v_mfma_f32_16x16x32_bf16 v[112:115], v[236:239], v[84:87], v[112:115]\n\t"
        "v_fma_f32 v110, v110, v38, v46\n\t"
        "v_mfma_f32_16x16x32_bf16 v[112:115], v[240:243], v[88:91], v[112:115]\n\t"
        "v_fma_f32 v111, v111, v39, v47\n\t"
        "v_mfma_f32_16x16x32_bf16 v[112:115], v[244:247], v[92:95], v[112:115]\n\t"
        "v_fmac_f32_dpp v108, v104, v36 row_shr:1 row_mask:0xf bank_mask:0xf bound_ctrl:1\n\t"
        "v_mfma_f32_16x16x32_bf16 v[112:115], v[248:251], v[96:99], v[112:115]\n\t"
        "v_fmac_f32_dpp v109, v105, v37 row_shr:1 row_mask:0xf bank_mask:0xf bound_ctrl:1\n\t"
        "v_mfma_f32_16x16x32_bf16 v[112:115], v[252:255], v[100:103], v[112:115]\n\t"
        "v_fmac_f32_dpp v110, v106, v38 row_shr:1 row_mask:0xf bank_mask:0xf bound_ctrl:1\n\t"
        "v_fmac_f32_dpp v111, v107, v39 row_shr:1 row_mask:0xf bank_mask:0xf bound_ctrl:1\n\t"
        "s_mov_b32 s63, 0x62000\n\t"
        "s_nop 6\n\t"
        "v_fmac_f32_dpp v108, v112, v36 row_shl:1 row_mask:0xf bank_mask:0xf bound_ctrl:1\n\t"
        "v_fmac_f32_dpp v109, v113, v37 row_shl:1 row_mask:0xf bank_mask:0xf bound_ctrl:1\n\t"
        "v_fmac_f32_dpp v110, v114, v38 row_shl:1 row_mask:0xf bank_mask:0xf bound_ctrl:1\n\t"
        "v_fmac_f32_dpp v111, v115, v39 row_shl:1 row_mask:0xf bank_mask:0xf bound_ctrl:1\n\t"
        "v_exp_f32 v0, v108\n\t"
        "v_exp_f32 v17, v109\n\t"
        "v_exp_f32 v18, v110\n\t"
        "v_exp_f32 v19, v111\n\t"
        "v_fma_f32 v0, v0, s61, s61\n\t"
        "v_fma_f32 v17, v17, s61, s61\n\t"
        "v_fma_f32 v18, v18, s61, s61\n\t"
        "v_fma_f32 v19, v19, s61, s61\n\t"
        "v_rcp_f32 v0, v0\n\t"
        "v_rcp_f32 v17, v17\n\t"
        "v_rcp_f32 v18, v18\n\t"
        "v_rcp_f32 v19, v19\n\t"
        "v_mul_f32 v108, v108, v0\n\t"
        "v_mul_f32 v109, v109, v17\n\t"
        "v_mul_f32 v110, v110, v18\n\t"
        "v_mul_f32 v111, v111, v19\n\t"
        "v_cvt_pk_bf16_f32 v24, v108, v109\n\t"
        "v_cvt_pk_bf16_f32 v25, v110, v111\n\t"
        "buffer_store_dwordx2 v[24:25], v16, %[osrd], s63 offen offset:8\n\t"
        "s_branch END%=\n\t"
        "HALF0%=:\n\t"
        "v_mfma_f32_16x16x32_bf16 v[104:107], a[0:3], v[48:51], 0\n\t"
        "v_mfma_f32_16x16x32_bf16 v[104:107], a[4:7], v[52:55], v[104:107]\n\t"
        "v_mfma_f32_16x16x32_bf16 v[104:107], a[8:11], v[56:59], v[104:107]\n\t"
        "v_mfma_f32_16x16x32_bf16 v[104:107], a[12:15], v[60:63], v[104:107]\n\t"
        "v_mfma_f32_16x16x32_bf16 v[104:107], a[16:19], v[64:67], v[104:107]\n\t"
        "v_fma_f32 v120, v120, v36, v44\n\t"
        "v_mfma_f32_16x16x32_bf16 v[104:107], a[20:23], v[68:71], v[104:107]\n\t"
        "v_fma_f32 v121, v121, v37, v45\n\t"
        "v_mfma_f32_16x16x32_bf16 v[104:107], a[24:27], v[72:75], v[104:107]\n\t"
        "s_waitcnt lgkmcnt(3)\n\t"
        "v_fma_f32 v122, v122, v38, v46\n\t"
        "v_mfma_f32_16x16x32_bf16 v[104:107], a[28:31], v[76:79], v[104:107]\n\t"
        "v_fma_f32 v123, v123, v39, v47\n\t"
        "v_mfma_f32_16x16x32_bf16 v[104:107], a[32:35], v[80:83], v[104:107]\n\t"
        "v_fmac_f32_dpp v120, v116, v36 row_shr:1 row_mask:0xf bank_mask:0xf bound_ctrl:1\n\t"
        "v_mfma_f32_16x16x32_bf16 v[104:107], a[36:39], v[84:87], v[104:107]\n\t"
        "v_fmac_f32_dpp v121, v117, v37 row_shr:1 row_mask:0xf bank_mask:0xf bound_ctrl:1\n\t"
        "v_mfma_f32_16x16x32_bf16 v[104:107], a[40:43], v[88:91], v[104:107]\n\t"
        "s_waitcnt lgkmcnt(0)\n\t"
        "v_fmac_f32_dpp v122, v118, v38 row_shr:1 row_mask:0xf bank_mask:0xf bound_ctrl:1\n\t"
        "v_mfma_f32_16x16x32_bf16 v[104:107], a[44:47], v[92:95], v[104:107]\n\t"
        "v_fmac_f32_dpp v123, v119, v39 row_shr:1 row_mask:0xf bank_mask:0xf bound_ctrl:1\n\t"
        "v_mfma_f32_16x16x32_bf16 v[104:107], a[48:51], v[96:99], v[104:107]\n\t"
        "v_fmac_f32_dpp v120, v124, v36 row_shl:1 row_mask:0xf bank_mask:0xf bound_ctrl:1\n\t"
        "v_mfma_f32_16x16x32_bf16 v[104:107], a[52:55], v[100:103], v[104:107]\n\t"
        "v_fmac_f32_dpp v121, v125, v37 row_shl:1 row_mask:0xf bank_mask:0xf bound_ctrl:1\n\t"
        "v_mfma_f32_16x16x32_bf16 v[108:111], a[56:59], v[48:51], 0\n\t"
        "v_fmac_f32_dpp v122, v126, v38 row_shl:1 row_mask:0xf bank_mask:0xf bound_ctrl:1\n\t"
        "v_mfma_f32_16x16x32_bf16 v[108:111], a[60:63], v[52:55], v[108:111]\n\t"
        "v_fmac_f32_dpp v123, v127, v39 row_shl:1 row_mask:0xf bank_mask:0xf bound_ctrl:1\n\t"
        "v_mfma_f32_16x16x32_bf16 v[108:111], a[64:67], v[56:59], v[108:111]\n\t"
        "v_exp_f32 v28, v120\n\t"
        "v_mfma_f32_16x16x32_bf16 v[108:111], a[68:71], v[60:63], v[108:111]\n\t"
        "v_exp_f32 v29, v121\n\t"
        "v_mfma_f32_16x16x32_bf16 v[108:111], a[72:75], v[64:67], v[108:111]\n\t"
        "v_exp_f32 v30, v122\n\t"
        "v_mfma_f32_16x16x32_bf16 v[108:111], a[76:79], v[68:71], v[108:111]\n\t"
        "v_exp_f32 v31, v123\n\t"
        "v_mfma_f32_16x16x32_bf16 v[108:111], a[80:83], v[72:75], v[108:111]\n\t"
        "v_fma_f32 v28, v28, s61, s61\n\t"
        "v_mfma_f32_16x16x32_bf16 v[108:111], a[84:87], v[76:79], v[108:111]\n\t"
        "v_fma_f32 v29, v29, s61, s61\n\t"
        "v_mfma_f32_16x16x32_bf16 v[108:111], a[88:91], v[80:83], v[108:111]\n\t"
        "v_fma_f32 v30, v30, s61, s61\n\t"
        "v_mfma_f32_16x16x32_bf16 v[108:111], a[92:95], v[84:87], v[108:111]\n\t"
        "v_fma_f32 v31, v31, s61, s61\n\t"
        "v_mfma_f32_16x16x32_bf16 v[108:111], a[96:99], v[88:91], v[108:111]\n\t"
        "v_rcp_f32 v28, v28\n\t"
        "v_mfma_f32_16x16x32_bf16 v[108:111], a[100:103], v[92:95], v[108:111]\n\t"
        "v_rcp_f32 v29, v29\n\t"
        "v_mfma_f32_16x16x32_bf16 v[108:111], a[104:107], v[96:99], v[108:111]\n\t"
        "v_rcp_f32 v30, v30\n\t"
        "v_mfma_f32_16x16x32_bf16 v[108:111], a[108:111], v[100:103], v[108:111]\n\t"
        "v_rcp_f32 v31, v31\n\t"
        "v_mfma_f32_16x16x32_bf16 v[112:115], a[112:115], v[48:51], 0\n\t"
        "v_mul_f32 v120, v120, v28\n\t"
        "v_mfma_f32_16x16x32_bf16 v[112:115], a[116:119], v[52:55], v[112:115]\n\t"
        "v_mul_f32 v121, v121, v29\n\t"
        "v_mfma_f32_16x16x32_bf16 v[112:115], a[120:123], v[56:59], v[112:115]\n\t"
        "v_mul_f32 v122, v122, v30\n\t"
        "v_mfma_f32_16x16x32_bf16 v[112:115], a[124:127], v[60:63], v[112:115]\n\t"
        "v_mul_f32 v123, v123, v31\n\t"
        "v_mfma_f32_16x16x32_bf16 v[112:115], a[128:131], v[64:67], v[112:115]\n\t"
        "v_cvt_pk_bf16_f32 v22, v120, v121\n\t"
        "v_mfma_f32_16x16x32_bf16 v[112:115], a[132:135], v[68:71], v[112:115]\n\t"
        "v_cvt_pk_bf16_f32 v23, v122, v123\n\t"
        "v_mfma_f32_16x16x32_bf16 v[112:115], a[136:139], v[72:75], v[112:115]\n\t"
        "buffer_store_dwordx4 v[20:23], v16, %[osrd], s53 offen\n\t"
        "v_mfma_f32_16x16x32_bf16 v[112:115], a[140:143], v[76:79], v[112:115]\n\t"
        "v_fma_f32 v108, v108, v32, v40\n\t"
        "v_mfma_f32_16x16x32_bf16 v[112:115], a[144:147], v[80:83], v[112:115]\n\t"
        "v_fma_f32 v109, v109, v33, v41\n\t"
        "v_mfma_f32_16x16x32_bf16 v[112:115], a[148:151], v[84:87], v[112:115]\n\t"
        "v_fma_f32 v110, v110, v34, v42\n\t"
        "v_mfma_f32_16x16x32_bf16 v[112:115], a[152:155], v[88:91], v[112:115]\n\t"
        "v_fma_f32 v111, v111, v35, v43\n\t"
        "v_mfma_f32_16x16x32_bf16 v[112:115], a[156:159], v[92:95], v[112:115]\n\t"
        "v_fmac_f32_dpp v108, v104, v32 row_shr:1 row_mask:0xf bank_mask:0xf bound_ctrl:1\n\t"
        "v_mfma_f32_16x16x32_bf16 v[112:115], a[160:163], v[96:99], v[112:115]\n\t"
        "v_fmac_f32_dpp v109, v105, v33 row_shr:1 row_mask:0xf bank_mask:0xf bound_ctrl:1\n\t"
        "v_mfma_f32_16x16x32_bf16 v[112:115], a[164:167], v[100:103], v[112:115]\n\t"
        "v_fmac_f32_dpp v110, v106, v34 row_shr:1 row_mask:0xf bank_mask:0xf bound_ctrl:1\n\t"
        "v_fmac_f32_dpp v111, v107, v35 row_shr:1 row_mask:0xf bank_mask:0xf bound_ctrl:1\n\t"
        "s_mov_b32 s63, 0x62000\n\t"
        "s_nop 6\n\t"
        "v_fmac_f32_dpp v108, v112, v32 row_shl:1 row_mask:0xf bank_mask:0xf bound_ctrl:1\n\t"
        "v_fmac_f32_dpp v109, v113, v33 row_shl:1 row_mask:0xf bank_mask:0xf bound_ctrl:1\n\t"
        "v_fmac_f32_dpp v110, v114, v34 row_shl:1 row_mask:0xf bank_mask:0xf bound_ctrl:1\n\t"
        "v_fmac_f32_dpp v111, v115, v35 row_shl:1 row_mask:0xf bank_mask:0xf bound_ctrl:1\n\t"
        "v_exp_f32 v0, v108\n\t"
        "v_exp_f32 v17, v109\n\t"
        "v_exp_f32 v18, v110\n\t"
        "v_exp_f32 v19, v111\n\t"
        "v_fma_f32 v0, v0, s61, s61\n\t"
        "v_fma_f32 v17, v17, s61, s61\n\t"
        "v_fma_f32 v18, v18, s61, s61\n\t"
        "v_fma_f32 v19, v19, s61, s61\n\t"
        "v_rcp_f32 v0, v0\n\t"
        "v_rcp_f32 v17, v17\n\t"
        "v_rcp_f32 v18, v18\n\t"
        "v_rcp_f32 v19, v19\n\t"
        "v_mul_f32 v108, v108, v0\n\t"
        "v_mul_f32 v109, v109, v17\n\t"
        "v_mul_f32 v110, v110, v18\n\t"
        "v_mul_f32 v111, v111, v19\n\t"
        "v_cvt_pk_bf16_f32 v24, v108, v109\n\t"
        "v_cvt_pk_bf16_f32 v25, v110, v111\n\t"
        "buffer_store_dwordx2 v[24:25], v16, %[osrd], s63 offen\n\t"
        "s_branch END%=\n\t"
        "TAIL0%=:\n\t"
        "s_nop 7\n\t"
        "v_fma_f32 v120, v120, v36, v44\n\t"
        "v_fma_f32 v121, v121, v37, v45\n\t"
        "v_fma_f32 v122, v122, v38, v46\n\t"
        "v_fma_f32 v123, v123, v39, v47\n\t"
        "v_fmac_f32_dpp v120, v116, v36 row_shr:1 row_mask:0xf bank_mask:0xf bound_ctrl:1\n\t"
        "v_fmac_f32_dpp v121, v117, v37 row_shr:1 row_mask:0xf bank_mask:0xf bound_ctrl:1\n\t"
        "v_fmac_f32_dpp v122, v118, v38 row_shr:1 row_mask:0xf bank_mask:0xf bound_ctrl:1\n\t"
        "v_fmac_f32_dpp v123, v119, v39 row_shr:1 row_mask:0xf bank_mask:0xf bound_ctrl:1\n\t"
        "v_fmac_f32_dpp v120, v124, v36 row_shl:1 row_mask:0xf bank_mask:0xf bound_ctrl:1\n\t"
        "v_fmac_f32_dpp v121, v125, v37 row_shl:1 row_mask:0xf bank_mask:0xf bound_ctrl:1\n\t"
        "v_fmac_f32_dpp v122, v126, v38 row_shl:1 row_mask:0xf bank_mask:0xf bound_ctrl:1\n\t"
        "v_fmac_f32_dpp v123, v127, v39 row_shl:1 row_mask:0xf bank_mask:0xf bound_ctrl:1\n\t"
        "v_exp_f32 v28, v120\n\t"
        "v_exp_f32 v29, v121\n\t"
        "v_exp_f32 v30, v122\n\t"
        "v_exp_f32 v31, v123\n\t"
        "v_fma_f32 v28, v28, s61, s61\n\t"
        "v_fma_f32 v29, v29, s61, s61\n\t"
        "v_fma_f32 v30, v30, s61, s61\n\t"
        "v_fma_f32 v31, v31, s61, s61\n\t"
        "v_rcp_f32 v28, v28\n\t"
        "v_rcp_f32 v29, v29\n\t"
        "v_rcp_f32 v30, v30\n\t"
        "v_rcp_f32 v31, v31\n\t"
        "v_mul_f32 v120, v120, v28\n\t"
        "v_mul_f32 v121, v121, v29\n\t"
        "v_mul_f32 v122, v122, v30\n\t"
        "v_mul_f32 v123, v123, v31\n\t"
        "v_cvt_pk_bf16_f32 v22, v120, v121\n\t"
        "v_cvt_pk_bf16_f32 v23, v122, v123\n\t"
        "buffer_store_dwordx4 v[20:23], v16, %[osrd], s53 offen\n\t"
        "END%=:\n\t"
        : [tid] "+v"(tid)
        : [wsrd] "s"(wsrd), [osrd] "s"(osrd), [xs0] "s"(xs0), [xs1] "s"(xs1), [xs2] "s"(xs2),
          [pg] "s"(pg), [pvar] "s"(pvar), [pmean] "s"(pmean), [pb] "s"(pb),
          [p0] "s"(p0), [wv] "s"(wv), [nld] "s"(nld), [extra] "s"(extra), [ci] "s"(ci),
          [lds] "s"((unsigned)(size_t)(__attribute__((address_space(3))) char*)lds)
        : "memory", "vcc", "scc", "v1", "v2", "v3", "v4", "v5", "v6", "v7", "v8", "v9", "v10", "v11", "v12", "v13", "v14", "v15", "v16", "v17", "v18", "v19", "v20", "v21", "v22", "v23", "v24", "v25", "v26", "v27", "v28", "v29", "v30", "v31", "v32", "v33", "v34", "v35", "v36", "v37", "v38", "v39", "v40", "v41", "v42", "v43", "v44", "v45", "v46", "v47", "v48", "v49", "v50", "v51", "v52", "v53", "v54", "v55", "v56", "v57", "v58", "v59", "v60", "v61", "v62", "v63", "v64", "v65", "v66", "v67", "v68", "v69", "v70", "v71", "v72", "v73", "v74", "v75", "v76", "v77", "v78", "v79", "v80", "v81", "v82", "v83", "v84", "v85", "v86", "v87", "v88", "v89", "v90", "v91", "v92", "v93", "v94", "v95", "v96", "v97", "v98", "v99", "v100", "v101", "v102", "v103", "v104", "v105", "v106", "v107", "v108", "v109", "v110", "v111", "v112", "v113", "v114", "v115", "v116", "v117", "v118", "v119", "v120", "v121", "v122", "v123", "v124", "v125", "v126", "v127", "v128", "v129", "v130", "v131", "v132", "v133", "v134", "v135", "v136", "v137", "v138", "v139", "v140", "v141", "v142", "v143", "v144", "v145", "v146", "v147", "v148", "v149", "v150", "v151", "v152", "v153", "v154", "v155", "v156", "v157", "v158", "v159", "v160", "v161", "v162", "v163", "v164", "v165", "v166", "v167", "v168", "v169", "v170", "v171", "v172", "v173", "v174", "v175", "v176", "v177", "v178", "v179", "v180", "v181", "v182", "v183", "v184", "v185", "v186", "v187", "v188", "v189", "v190", "v191", "v192", "v193", "v194", "v195", "v196", "v197", "v198", "v199", "v200", "v201", "v202", "v203", "v204", "v205", "v206", "v207", "v208", "v209", "v210", "v211", "v212", "v213", "v214", "v215", "v216", "v217", "v218", "v219", "v220", "v221", "v222", "v223", "v224", "v225", "v226", "v227", "v228", "v229", "v230", "v231", "v232", "v233", "v234", "v235", "v236", "v237", "v238", "v239", "v240", "v241", "v242", "v243", "v244", "v245", "v246", "v247", "v248", "v249", "v250", "v251", "v252", "v253", "v254", "v255", "a0", "a1", "a2", "a3", "a4", "a5", "a6", "a7", "a8", "a9", "a10", "a11", "a12", "a13", "a14", "a15", "a16", "a17", "a18", "a19", "a20", "a21", "a22", "a23", "a24", "a25", "a26", "a27", "a28", "a29", "a30", "a31", "a32", "a33", "a34", "a35", "a36", "a37", "a38", "a39", "a40", "a41", "a42", "a43", "a44", "a45", "a46", "a47", "a48", "a49", "a50", "a51", "a52", "a53", "a54", "a55", "a56", "a57", "a58", "a59", "a60", "a61", "a62", "a63", "a64", "a65", "a66", "a67", "a68", "a69", "a70", "a71", "a72", "a73", "a74", "a75", "a76", "a77", "a78", "a79", "a80", "a81", "a82", "a83", "a84", "a85", "a86", "a87", "a88", "a89", "a90", "a91", "a92", "a93", "a94", "a95", "a96", "a97", "a98", "a99", "a100", "a101", "a102", "a103", "a104", "a105", "a106", "a107", "a108", "a109", "a110", "a111", "a112", "a113", "a114", "a115", "a116", "a117", "a118", "a119", "a120", "a121", "a122", "a123", "a124", "a125", "a126", "a127", "a128", "a129", "a130", "a131", "a132", "a133", "a134", "a135", "a136", "a137", "a138", "a139", "a140", "a141", "a142", "a143", "a144", "a145", "a146", "a147", "a148", "a149", "a150", "a151", "a152", "a153", "a154", "a155", "a156", "a157", "a158", "a159", "a160", "a161", "a162", "a163", "a164", "a165", "a166", "a167", "a168", "a169", "a170", "a171", "a172", "a173", "a174", "a175", "a176", "a177", "a178", "a179", "a180", "a181", "a182", "a183", "a184", "a185", "a186", "a187", "a188", "a189", "a190", "a191", "a192", "a193", "a194", "a195", "a196", "a197", "a198", "a199", "a200", "a201", "a202", "a203", "a204", "a205", "a206", "a207", "a208", "a209", "a210", "a211", "a212", "a213", "a214", "a215", "a216", "a217", "a218", "a219", "a220", "a221", "a222", "a223", "a224", "a225", "a226", "a227", "a228", "a229", "a230", "a231", "a232", "a233", "a234", "a235", "a236", "a237", "a238", "a239", "a240", "a241", "a242", "a243", "a244", "a245", "a246", "a247", "a248", "a249", "a250", "a251", "a252", "a253", "a254", "a255", "s36", "s37", "s38", "s39", "s40", "s41", "s42", "s43", "s44", "s45", "s46", "s47", "s48", "s49", "s50", "s51", "s52", "s53", "s54", "s55", "s56", "s57", "s58", "s59", "s60", "s61", "s62", "s63", "s64", "s65", "s66", "s67", "s68", "s69", "s70", "s71");

}

extern "C" void kernel_launch(void* const* d_in, const int* in_sizes, int n_in,
                              void* d_out, int out_size, void* d_ws, size_t ws_size,
                              hipStream_t stream) {
    conv_bn_silu_kernel<<<256, 256, 0, stream>>>((const char*)d_in[0], (const char*)d_in[1], (const char*)d_in[2],
                                                 (const char*)d_in[3], (const char*)d_in[4], (const char*)d_in[5],
                                                 (char*)d_out);
}
```
